```python
import torch

def _softmax(x, axis=-1):
    return torch.softmax(x, dim=axis)
def _gelu(x):
    return torch.nn.functional.gelu(x, approximate="tanh")
def _silu(x):
    return torch.nn.functional.silu(x)
def _sigmoid(x):
    return torch.sigmoid(x)
def _layernorm(x, axis=-1, eps=1e-5):
    m = x.mean(dim=axis, keepdim=True)
    v = x.var(dim=axis, keepdim=True, unbiased=False)
    return (x - m) / torch.sqrt(v + eps)
def _scatter_add(tgt, idx, src, axis=0):
    return tgt.clone().index_add_(axis, idx, src)
def _segment_sum(src, seg, n):
    out = torch.zeros((n,) + src.shape[1:], dtype=src.dtype)
    return out.index_add_(0, seg, src)
def _pad(x, pw):
    flat = tuple(v for pair in reversed(pw) for v in pair)
    return torch.nn.functional.pad(x, flat)

kernel_name = 'standalone__paged_block_table_kv_pagedattention__sampling'

def setup_inputs():

    _w = (lambda a: (lambda t: t.to(torch.bfloat16) if t.is_floating_point() else t)(torch.as_tensor(a)))
    B, Hq, Hkv, Sq, Skv, D = 8, 32, 4, 32, 8192, 128
    bs = 16
    nbps = max(1, Skv // bs)
    num_phys = B * nbps + 4
    bt = torch.randperm(num_phys)[: B * nbps].reshape(B, nbps).to(torch.int64)
    cl = (nbps * bs - (torch.arange(1, B + 1, dtype=torch.int64) * 2654435761) % max(1, nbps * bs // 2)).to(torch.int64)
    return {
        "q": _w((lambda *s: torch.randn(*s, dtype=torch.bfloat16) * 0.8)(B, Hq, Sq, D)),
        "k_cache": _w((lambda *s: torch.randn(*s, dtype=torch.bfloat16) * 0.8)(num_phys, bs, Hkv, D)),
        "v_cache": _w((lambda *s: torch.randn(*s, dtype=torch.bfloat16) * 0.05)(num_phys, bs, Hkv, D)),
        "block_table": _w(bt),
        "context_lens": _w(cl),
    }

def reference(q, k_cache, v_cache, block_table, context_lens):
    B, Hq, Sq, D = q.shape
    bs = k_cache.shape[1]
    Hkv = k_cache.shape[2]
    nbps = block_table.shape[1]
    Skv = nbps * bs
    bt = block_table.reshape(-1)
    k = (lambda x, *d: x.permute(*d))(k_cache[bt].reshape(B, Skv, Hkv, D), 0, 2, 1, 3)
    v = (lambda x, *d: x.permute(*d))(v_cache[bt].reshape(B, Skv, Hkv, D), 0, 2, 1, 3)
    g = Hq // Hkv
    qg = q.reshape(B, Hkv, g, Sq, D)
    scores = torch.einsum("bhgqd,bhkd->bhgqk", qg, k) * (D ** -0.5)
    pos = torch.arange(Skv)
    msk = pos[None, :] < context_lens[:, None]
    scores = torch.where(msk[:, None, None, None, :], scores, -1e9)
    p = _softmax(scores, axis=-1)
    out = torch.einsum("bhgqk,bhkd->bhgqd", p, v)
    return out.reshape(B, Hq, Sq, D)
```

```cpp
#include <hip/hip_runtime.h>
#include <hip/hip_bf16.h>
#include <stdint.h>

constexpr int B = 8, HQ = 32, HKV = 4, SQ = 32, SKV = 8192, D = 128, PG = 16, NBPS = SKV / PG, G = HQ / HKV;

__device__ __forceinline__ float bf_lo(unsigned u) { return __uint_as_float(u << 16); }
__device__ __forceinline__ float bf_hi(unsigned u) { return __uint_as_float(u & 0xffff0000u); }
__device__ __forceinline__ float wave_max(float x) { for (int o = 32; o; o >>= 1) x = fmaxf(x, __shfl_xor(x, o, 64)); return x; }
__device__ __forceinline__ float wave_sum(float x) { for (int o = 32; o; o >>= 1) x += __shfl_xor(x, o, 64); return x; }

__device__ void slow_row(const unsigned short* __restrict__ q, const unsigned short* __restrict__ kc, const unsigned short* __restrict__ vc,
                         const int* __restrict__ bt, int len, int b, int kvh, size_t row, unsigned short* __restrict__ out, int lane) {
  const unsigned qv = *reinterpret_cast<const unsigned*>(q + row * D + 2 * lane);
  const float L2E = 1.4426950408889634f, SC = 0.088388347648318447f;
  float m = -INFINITY, l = 0.f, o0 = 0.f, o1 = 0.f;
  for (int k0 = 0; k0 < SKV; k0 += 64) {
    const int key = k0 + lane;
    const int page = bt[b * NBPS + (key >> 4)];
    const size_t rowoff = (((size_t)page * PG + (key & 15)) * HKV + kvh) * D;
    float s = -1e9f;
    if (k0 < len) {
      float acc = 0.f;
      for (int c = 0; c < 16; ++c) {
        const uint4 kk = *reinterpret_cast<const uint4*>(kc + rowoff + c * 8);
        const unsigned kw[4] = {kk.x, kk.y, kk.z, kk.w};
#pragma unroll
        for (int e = 0; e < 4; ++e) {
          const unsigned qq = __shfl(qv, c * 4 + e, 64);
          acc = fmaf(bf_lo(qq), bf_lo(kw[e]), acc); acc = fmaf(bf_hi(qq), bf_hi(kw[e]), acc);
        }
      }
      s = key < len ? acc * SC : -1e9f;
    } else if (m > -9.0e8f) break;
    const float mn = fmaxf(m, wave_max(s));
    const float alpha = exp2f((m - mn) * L2E), p = exp2f((s - mn) * L2E);
    l = l * alpha + wave_sum(p); o0 *= alpha; o1 *= alpha; m = mn;
    for (int j = 0; j < 64; ++j) {
      const float pj = __shfl(p, j, 64);
      const size_t ro = __shfl((unsigned)(rowoff >> 7), j, 64);
      const unsigned vv = *reinterpret_cast<const unsigned*>(vc + ro * 128 + 2 * lane);
      o0 = fmaf(pj, bf_lo(vv), o0); o1 = fmaf(pj, bf_hi(vv), o1);
    }
  }
  const float inv = 1.f / l;
  __hip_bfloat16 r0 = __float2bfloat16(o0 * inv), r1 = __float2bfloat16(o1 * inv);
  unsigned w = (unsigned)(*reinterpret_cast<unsigned short*>(&r0)) | ((unsigned)(*reinterpret_cast<unsigned short*>(&r1)) << 16);
  *reinterpret_cast<unsigned*>(out + row * D + 2 * lane) = w;
}

__global__ __launch_bounds__(256) void naive_kernel(const unsigned short* q, const unsigned short* kc, const unsigned short* vc, const int* bt, const int* lens, unsigned short* out) {
  const int lane = threadIdx.x & 63, w = threadIdx.x >> 6;
  const size_t row = (size_t)blockIdx.x * 4 + w;
  const int b = row / (HQ * SQ), kvh = (row / SQ % HQ) / G;
  slow_row(q, kc, vc, bt, lens[b], b, kvh, row, out, lane);
}

extern "C" void kernel_launch(void* const* d_in, const int* in_sizes, int n_in, void* d_out, int out_size, void* d_ws, size_t ws_size, hipStream_t stream) {
  naive_kernel<<<B * HQ * SQ / 4, 256, 0, stream>>>((const unsigned short*)d_in[0], (const unsigned short*)d_in[1], (const unsigned short*)d_in[2],
                                                   (const int*)d_in[3], (const int*)d_in[4], (unsigned short*)d_out);
}
```

```cpp
#include <hip/hip_runtime.h>
#include <hip/hip_bf16.h>
#include <stdint.h>
#define HSH 1

constexpr int B = 8, HQ = 32, HKV = 4, SQ = 32, SKV = 8192, D = 128, PG = 16, NBPS = SKV / PG, G = HQ / HKV;
constexpr unsigned WAVE_PART = 17408u, SLOT_PART = 4u * WAVE_PART;
constexpr unsigned FLG_OFF = 0x1200000u;
constexpr size_t LDS_BYTES = 163840;
typedef unsigned u32x4 __attribute__((ext_vector_type(4)));
typedef unsigned u32x2 __attribute__((ext_vector_type(2)));
typedef float f32x2 __attribute__((ext_vector_type(2)));
typedef __bf16 bf16x2 __attribute__((ext_vector_type(2)));
typedef __attribute__((address_space(1))) unsigned long long gu64;
#define DI __device__ __forceinline__
#define RLX_AGENT __ATOMIC_RELAXED, __HIP_MEMORY_SCOPE_AGENT
constexpr int AUX_SC1 = 16;
DI unsigned rfl(unsigned x) { return __builtin_amdgcn_readfirstlane(x); }
DI float bf_lo(unsigned u) { return __uint_as_float(u << 16); }
DI float bf_hi(unsigned u) { return __uint_as_float(u & 0xffff0000u); }
DI unsigned cvtpk(float lo, float hi) { f32x2 v = {lo, hi}; bf16x2 b = __builtin_convertvector(v, bf16x2); return __builtin_bit_cast(unsigned, b); }
DI float wave_max(float x) { for (int o = 32; o; o >>= 1) x = fmaxf(x, __shfl_xor(x, o, 64)); return x; }
DI float wave_sum(float x) { for (int o = 32; o; o >>= 1) x += __shfl_xor(x, o, 64); return x; }

DI void slow_row(const unsigned short* __restrict__ q, const unsigned short* __restrict__ kc, const unsigned short* __restrict__ vc,
                 const int* __restrict__ bt, int len, int b, int kvh, size_t row, unsigned short* __restrict__ out, int lane, int dlo) {
  const unsigned qv = *reinterpret_cast<const unsigned*>(q + row * D + 2 * lane);
  const float L2E = 1.4426950408889634f, SC = 0.088388347648318447f;
  float m = -INFINITY, l = 0.f, o0 = 0.f, o1 = 0.f;
  for (int k0 = 0; k0 < SKV; k0 += 64) {
    const int key = k0 + lane;
    const int page = bt[b * NBPS + (key >> 4)];
    const size_t rowoff = (((size_t)page * PG + (key & 15)) * HKV + kvh) * D;
    float s = -1e9f;
    if (k0 < len) {
      float acc = 0.f;
#pragma unroll 1
      for (int c = 0; c < 16; ++c) {
        const uint4 kk = *reinterpret_cast<const uint4*>(kc + rowoff + c * 8);
        const unsigned kw[4] = {kk.x, kk.y, kk.z, kk.w};
#pragma unroll
        for (int e = 0; e < 4; ++e) {
          const unsigned qq = __shfl(qv, c * 4 + e, 64);
          acc = fmaf(bf_lo(qq), bf_lo(kw[e]), acc); acc = fmaf(bf_hi(qq), bf_hi(kw[e]), acc);
        }
      }
      s = key < len ? acc * SC : -1e9f;
    } else if (m > -9.0e8f) break;
    const float mn = fmaxf(m, wave_max(s));
    const float alpha = exp2f((m - mn) * L2E), p = exp2f((s - mn) * L2E);
    l = l * alpha + wave_sum(p); o0 *= alpha; o1 *= alpha; m = mn;
#pragma unroll 4
    for (int j = 0; j < 64; ++j) {
      const float pj = __shfl(p, j, 64);
      const size_t ro = __shfl((unsigned)(rowoff >> 7), j, 64);
      const unsigned vv = *reinterpret_cast<const unsigned*>(vc + ro * 128 + 2 * lane);
      o0 = fmaf(pj, bf_lo(vv), o0); o1 = fmaf(pj, bf_hi(vv), o1);
    }
  }
  const float inv = 1.f / l;
  if (2 * lane >= dlo && 2 * lane < dlo + 32) *reinterpret_cast<unsigned*>(out + row * D + 2 * lane) = cvtpk(o0 * inv, o1 * inv);
}

DI unsigned combine_unit(__amdgpu_buffer_rsrc_t srd, int n, unsigned i, unsigned lane, bool allbad, unsigned short* __restrict__ outw) {
  constexpr int CH = 8;
  float M = -INFINITY, L = 0.f, o[8];
  bool bad = allbad;
#pragma unroll
  for (int e = 0; e < 8; ++e) o[e] = 0.f;
  for (int j0 = 0; j0 < n; j0 += CH) {
    u32x4 po[CH]; u32x2 ml[CH];
#pragma unroll
    for (int j = 0; j < CH; ++j) ml[j] = __builtin_amdgcn_raw_buffer_load_b64(srd, (lane & 15u) * 8u + (unsigned)j * SLOT_PART, 16384u + (i >> 2) * 128u + (unsigned)j0 * SLOT_PART, AUX_SC1);
#pragma unroll
    for (int j = 0; j < CH; ++j) po[j] = __builtin_amdgcn_raw_buffer_load_b128(srd, lane * 16u + (unsigned)j * SLOT_PART, i * 1024u + (unsigned)j0 * SLOT_PART, AUX_SC1);
    float Mn = M;
#pragma unroll
    for (int j = 0; j < CH; ++j) {
      Mn = j0 + j < n ? fmaxf(Mn, -__uint_as_float(ml[j][0])) : Mn;
      bad |= j0 + j < n && !(__uint_as_float(ml[j][1]) <= 1.0e18f);
    }
    const float al = __builtin_amdgcn_exp2f(M - Mn);
    L *= al;
#pragma unroll
    for (int e = 0; e < 8; ++e) o[e] *= al;
    M = Mn;
#pragma unroll
    for (int j = 0; j < CH; ++j) {
      const bool in = j0 + j < n;
      const float wj = in ? __builtin_amdgcn_exp2f(-__uint_as_float(ml[j][0]) - Mn) : 0.f;
      L = fmaf(wj, in ? __uint_as_float(ml[j][1]) : 0.f, L);
#pragma unroll
      for (int e = 0; e < 4; ++e) { const unsigned x = in ? po[j][e] : 0u; o[2 * e] = fmaf(wj, bf_lo(x), o[2 * e]); o[2 * e + 1] = fmaf(wj, bf_hi(x), o[2 * e + 1]); }
    }
  }
  const float inv = __builtin_amdgcn_rcpf(L);
  bool bd = bad || !(M > -1.0e9f);
#pragma unroll
  for (int e = 0; e < 8; ++e) { o[e] *= inv; bd |= !(fabsf(o[e]) <= 3.0e38f); }
  const unsigned a0 = cvtpk(o[0], o[1]), a1 = cvtpk(o[2], o[3]), b0 = cvtpk(o[4], o[5]), b1 = cvtpk(o[6], o[7]);
  const auto r0 = __builtin_amdgcn_permlane16_swap(a0, b0, false, false);
  const auto r1 = __builtin_amdgcn_permlane16_swap(a1, b1, false, false);
  const unsigned long long bb = __ballot(bd);
  const unsigned b2 = (unsigned)bb | (unsigned)(bb >> 32), bm = (b2 | (b2 >> 16)) & 0xffffu;
  if (!((bm >> (lane & 15u)) & 1u)) {
    const u32x4 v = {r0[0], r1[0], r0[1], r1[1]};
    *reinterpret_cast<u32x4*>(outw + ((i >> 2) * 16u + (lane & 15u)) * D + (i & 3u) * 32u + (lane & 16u) + 8u * (lane >> 5)) = v;
  }
  return bm;
}

__global__ __launch_bounds__(256, 1) void attn_fused(const unsigned short* __restrict__ q, const unsigned short* __restrict__ kc, const unsigned short* __restrict__ vc,
                                                      const int* __restrict__ bt, const int* __restrict__ lens, char* __restrict__ ws, unsigned short* __restrict__ out) {
  int b, n, j, len, slot, nt, mode;
  unsigned e0, e1, b0, b1, b2, b3;
  asm volatile(
      "s_load_dwordx2 s[56:57], %[ka], 32\n\t"
      "s_load_dwordx2 s[64:65], %[ka], 24\n\t"
      "s_load_dwordx2 s[36:37], %[ka], 8\n\t"
      "s_load_dwordx2 s[72:73], %[ka], 16\n\t"
      "s_load_dwordx2 s[60:61], %[ka], 0\n\t"
      "s_load_dwordx2 s[76:77], %[ka], 40\n\t"
      "s_load_dwordx2 s[82:83], %[ka], 48\n\t"
      "v_lshlrev_b32 v43, 6, %[tid]\n\t"
      "v_and_b32 v40, 7, %[tid]\n\t"
      "v_lshlrev_b32 v40, 2, v40\n\t"
      "v_lshrrev_b32 v41, 6, %[tid]\n\t"
      "v_and_b32 v32, 63, %[tid]\n\t"
      "v_readfirstlane_b32 s43, v41\n\t"
      "s_bfe_u32 s42, %[bid], 131073\n\t"
      "s_lshr_b32 s41, %[bid], 2\n\t"
      "s_andn2_b32 s41, s41, 1\n\t"
      "s_and_b32 s48, %[bid], 1\n\t"
      "s_or_b32 s41, s41, s48\n\t"
      "s_lshr_b32 s59, %[bid], 3\n\t"
      "s_lshl2_add_u32 s59, s59, s43\n\t"
      "s_lshr_b32 s48, s59, 4\n\t"
      "s_lshl2_add_u32 s48, s48, s42\n\t"
      "s_lshl_b32 s48, s48, 4\n\t"
      "s_and_b32 s59, s59, 15\n\t"
      "s_add_u32 s48, s48, s59\n\t"
      "s_lshl_b32 s48, s48, 12\n\t"
      "v_lshl_add_u32 v25, v32, 6, s48\n\t"
      "v_lshlrev_b32 v1, 4, v32\n\t"
      "v_lshrrev_b32 v33, 3, v32\n\t"
      "v_and_b32 v34, 7, v32\n\t"
      "v_lshrrev_b32 v35, 4, v32\n\t"
      "v_lshrrev_b32 v36, 5, v32\n\t"
      "v_and_b32 v45, 1, v33\n\t"
      "v_xor_b32 v45, 1, v45\n\t"
      "v_lshlrev_b32 v44, 13, v45\n\t"
      "v_xor_b32 v37, v34, v35\n\t"
      "v_lshlrev_b32 v37, 4, v37\n\t"
      "v_add_u32 v37, 0x1000, v37\n\t"
      "v_xor_b32 v38, 64, v37\n\t"
      "v_lshl_add_u32 v56, v33, 8, v37\n\t"
      "v_lshl_add_u32 v58, v33, 8, v38\n\t"
      "v_add_u32 v57, 0xfffffc80, v56\n\t"
      "v_add_u32 v59, 0xfffffc80, v58\n\t"
      "v_lshl_add_u32 v7, v33, 10, v37\n\t"
      "v_lshl_add_u32 v9, v33, 10, v38\n\t"
      "v_add_u32 v9, 6144, v9\n\t"
      "v_add_u32 v8, 0xfffffc80, v7\n\t"
      "v_add_u32 v10, 0xfffffc80, v9\n\t"
      "v_bfe_u32 v39, v32, 2, 3\n\t"
      "v_lshlrev_b32 v41, 1, v35\n\t"
      "v_xor_b32 v41, v41, v32\n\t"
      "v_and_b32 v41, 3, v41\n\t"
      "v_lshlrev_b32 v11, 10, v39\n\t"
      "v_lshl_add_u32 v11, v36, 6, v11\n\t"
      "v_lshl_add_u32 v11, v41, 4, v11\n\t"
      "v_add_u32 v11, 0x1000, v11\n\t"
      "v_add_u32 v12, 0xfffffc80, v11\n\t"
      "v_add_u32 v13, 6144, v11\n\t"
      "v_add_u32 v14, 5248, v11\n\t"
      "v_lshlrev_b32 v46, 2, v34\n\t"
      "v_mov_b32 v47, 0x88888888\n\t"
      "v_bfe_u32 v46, v47, v46, 4\n\t"
      "v_lshlrev_b32 v47, 2, v35\n\t"
      "v_mov_b32 v39, 0x9654\n\t"
      "v_bfe_u32 v47, v39, v47, 4\n\t"
      "v_add3_u32 v46, v46, v47, -12\n\t"
      "s_waitcnt lgkmcnt(0)\n\t"
      "global_load_dword v42, v40, s[56:57]\n\t"
      "global_load_dword v27, v43, s[64:65]\n\t"
      "global_load_dword v24, v25, s[60:61]\n\t"
      "s_lshl_b32 s48, s42, 8\n\t"
      "s_sub_u32 s59, 4096, s48\n\t"
      "s_sub_u32 s36, s36, s59\n\t"
      "s_subb_u32 s37, s37, 0\n\t"
      "s_add_u32 s38, s59, 67174400\n\t"
      "s_mov_b32 s39, 0x20000\n\t"
      "s_sub_u32 s72, s72, s59\n\t"
      "s_subb_u32 s73, s73, 0\n\t"
      "s_add_u32 s74, s59, 67174400\n\t"
      "s_mov_b32 s75, 0x20000\n\t"
      "s_lshl_b32 s49, s43, 12\n\t"
      "s_lshl_b32 s48, s42, 16\n\t"
      "s_lshl_b32 s59, s43, 14\n\t"
      "s_add_u32 s48, s48, s59\n\t"
      "s_add_u32 s60, s60, s48\n\t"
      "s_addc_u32 s61, s61, 0\n\t"
      "s_add_u32 s82, s82, s48\n\t"
      "s_addc_u32 s83, s83, 0\n\t"
      "s_sub_u32 s60, s60, 4096\n\t"
      "s_subb_u32 s61, s61, 0\n\t"
      "s_mov_b32 s62, 20480\n\t"
      "s_mov_b32 s63, 0x20000\n\t"
      "s_mov_b32 s67, 0x20000\n\t"
      "s_add_u32 s84, s76, 18874368\n\t"
      "s_addc_u32 s85, s77, 0\n\t"
      "s_lshl_b32 s48, s42, 6\n\t"
      "s_add_u32 s48, s48, s41\n\t"
      "s_mul_i32 s48, s48, 69632\n\t"
      "s_mul_i32 s59, s43, 17408\n\t"
      "s_add_u32 s48, s48, s59\n\t"
      "s_add_u32 s76, s76, s48\n\t"
      "s_addc_u32 s77, s77, 0\n\t"
      "s_mov_b32 s78, 17408\n\t"
      "s_mov_b32 s79, 0x20000\n\t"
      "s_mov_b32 s50, 0x00800080\n\t"
      "s_mov_b32 s51, 0x00800080\n\t"
      "v_bfe_u32 v41, v32, 1, 3\n\t"
      "v_xor_b32 v41, v41, v35\n\t"
      "v_lshlrev_b32 v2, 7, v34\n\t"
      "v_lshl_add_u32 v2, v41, 4, v2\n\t"
      "v_and_b32 v41, 1, v33\n\t"
      "v_lshl_add_u32 v2, v41, 11, v2\n\t"
      "v_xor_b32 v3, 64, v2\n\t"
      "v_add_u32 v60, s49, v2\n\t"
      "v_add_u32 v60, 0x10000, v60\n\t"
      "v_xor_b32 v61, 64, v60\n\t"
      "v_add_u32 v62, 0x10000, v60\n\t"
      "v_add_u32 v63, 0x10000, v61\n\t"
      "v_and_b32 v41, 3, v32\n\t"
      "v_lshlrev_b32 v4, 3, v41\n\t"
      "v_bfe_u32 v41, v32, 2, 2\n\t"
      "v_lshl_add_u32 v4, v41, 6, v4\n\t"
      "v_lshl_add_u32 v4, v36, 11, v4\n\t"
      "v_and_b32 v41, 1, v35\n\t"
      "v_mul_u32_u24 v41, 0x120, v41\n\t"
      "v_add_u32 v4, v4, v41\n\t"
      "v_add_u32 v4, 81920, v4\n\t"
      "v_xor_b32 v5, 32, v4\n\t"
      "v_bfe_u32 v41, v32, 1, 3\n\t"
      "v_lshlrev_b32 v39, 1, v36\n\t"
      "v_sub_u32 v41, v41, v39\n\t"
      "v_mov_b32 v39, 0x3f803f80\n\t"
      "v_cmp_eq_u32_e32 vcc, 0, v41\n\t"
      "v_cmp_eq_u32_e64 s[52:53], 4, v41\n\t"
      "v_and_b32 v6, 1, v32\n\t"
      "v_cndmask_b32_e32 v37, 0, v39, vcc\n\t"
      "v_mul_u32_u24 v6, 0xaaaa, v6\n\t"
      "v_cndmask_b32_e64 v38, 0, v39, s[52:53]\n\t"
      "v_or_b32 v6, 0x4444, v6\n\t"
      "v_accvgpr_write_b32 a192, v37\n\t"
      "v_accvgpr_write_b32 a196, v38\n\t"
      "v_accvgpr_write_b32 a193, v37\n\t"
      "v_accvgpr_write_b32 a197, v38\n\t"
      "v_accvgpr_write_b32 a194, v37\n\t"
      "v_accvgpr_write_b32 a198, v38\n\t"
      "v_accvgpr_write_b32 a195, v37\n\t"
      "v_accvgpr_write_b32 a199, v38\n\t"
      "v_mov_b32 v20, 0xff800000\n\t"
      "v_mov_b64 v[48:49], 0\n\t"
      "v_mov_b64 v[50:51], 0\n\t"
      "v_mov_b64 v[128:129], 0\n\t"
      "v_mov_b64 v[130:131], 0\n\t"
      "v_mov_b64 v[132:133], 0\n\t"
      "v_mov_b64 v[134:135], 0\n\t"
      "v_mov_b64 v[136:137], 0\n\t"
      "v_mov_b64 v[138:139], 0\n\t"
      "v_mov_b64 v[140:141], 0\n\t"
      "v_mov_b64 v[142:143], 0\n\t"
      "v_mov_b64 v[144:145], 0\n\t"
      "v_mov_b64 v[146:147], 0\n\t"
      "v_mov_b64 v[148:149], 0\n\t"
      "v_mov_b64 v[150:151], 0\n\t"
      "v_mov_b64 v[152:153], 0\n\t"
      "v_mov_b64 v[154:155], 0\n\t"
      "v_mov_b64 v[156:157], 0\n\t"
      "v_mov_b64 v[158:159], 0\n\t"
      "v_mov_b64 v[160:161], 0\n\t"
      "v_mov_b64 v[162:163], 0\n\t"
      "v_mov_b64 v[164:165], 0\n\t"
      "v_mov_b64 v[166:167], 0\n\t"
      "v_mov_b64 v[168:169], 0\n\t"
      "v_mov_b64 v[170:171], 0\n\t"
      "v_mov_b64 v[172:173], 0\n\t"
      "v_mov_b64 v[174:175], 0\n\t"
      "v_mov_b64 v[176:177], 0\n\t"
      "v_mov_b64 v[178:179], 0\n\t"
      "v_mov_b64 v[180:181], 0\n\t"
      "v_mov_b64 v[182:183], 0\n\t"
      "v_mov_b64 v[184:185], 0\n\t"
      "v_mov_b64 v[186:187], 0\n\t"
      "v_mov_b64 v[188:189], 0\n\t"
      "v_mov_b64 v[190:191], 0\n\t"
      "v_mov_b64 v[192:193], 0\n\t"
      "v_mov_b64 v[194:195], 0\n\t"
      "v_mov_b64 v[196:197], 0\n\t"
      "v_mov_b64 v[198:199], 0\n\t"
      "v_mov_b64 v[200:201], 0\n\t"
      "v_mov_b64 v[202:203], 0\n\t"
      "v_mov_b64 v[204:205], 0\n\t"
      "v_mov_b64 v[206:207], 0\n\t"
      "v_mov_b64 v[208:209], 0\n\t"
      "v_mov_b64 v[210:211], 0\n\t"
      "v_mov_b64 v[212:213], 0\n\t"
      "v_mov_b64 v[214:215], 0\n\t"
      "v_mov_b64 v[216:217], 0\n\t"
      "v_mov_b64 v[218:219], 0\n\t"
      "v_mov_b64 v[220:221], 0\n\t"
      "v_mov_b64 v[222:223], 0\n\t"
      "v_mov_b64 v[224:225], 0\n\t"
      "v_mov_b64 v[226:227], 0\n\t"
      "v_mov_b64 v[228:229], 0\n\t"
      "v_mov_b64 v[230:231], 0\n\t"
      "v_mov_b64 v[232:233], 0\n\t"
      "v_mov_b64 v[234:235], 0\n\t"
      "v_mov_b64 v[236:237], 0\n\t"
      "v_mov_b64 v[238:239], 0\n\t"
      "v_mov_b64 v[240:241], 0\n\t"
      "v_mov_b64 v[242:243], 0\n\t"
      "v_mov_b64 v[244:245], 0\n\t"
      "v_mov_b64 v[246:247], 0\n\t"
      "v_mov_b64 v[248:249], 0\n\t"
      "v_mov_b64 v[250:251], 0\n\t"
      "v_mov_b64 v[252:253], 0\n\t"
      "v_mov_b64 v[254:255], 0\n\t"
      "s_waitcnt vmcnt(2)\n\t"
      "v_med3_i32 v64, v42, 0, v44\n\t"
      "v_add_u32 v65, 63, v64\n\t"
      "v_lshrrev_b32 v65, 6, v65\n\t"
      "s_nop 1\n\t"
      "v_add_u32_dpp v66, v65, v65 row_shr:1 row_mask:0xf bank_mask:0xf bound_ctrl:0\n\t"
      "s_nop 1\n\t"
      "v_add_u32_dpp v66, v66, v66 row_shr:2 row_mask:0xf bank_mask:0xf bound_ctrl:0\n\t"
      "s_nop 1\n\t"
      "v_add_u32_dpp v66, v66, v66 row_shr:4 row_mask:0xf bank_mask:0xf bound_ctrl:0\n\t"
      "s_nop 0\n\t"
      "v_readlane_b32 s44, v66, 7\n\t"
      "s_add_u32 s44, s44, 63\n\t"
      "s_lshr_b32 s44, s44, 6\n\t"
      "s_max_u32 s44, s44, 1\n\t"
      "v_add_u32 v67, s44, v46\n\t"
      "v_max_i32 v67, 1, v67\n\t"
      "v_cvt_f32_u32 v68, v67\n\t"
      "v_rcp_f32 v68, v68\n\t"
      "v_add3_u32 v69, v65, v67, -1\n\t"
      "v_cvt_f32_u32 v69, v69\n\t"
      "v_fmaak_f32 v69, v69, v68, 0x3d000000\n\t"
      "v_cvt_u32_f32 v69, v69\n\t"
      "v_max_u32 v69, v69, v45\n\t"
      "s_nop 1\n\t"
      "v_add_u32_dpp v70, v69, v69 row_shr:1 row_mask:0xf bank_mask:0xf bound_ctrl:0\n\t"
      "s_nop 1\n\t"
      "v_add_u32_dpp v70, v70, v70 row_shr:2 row_mask:0xf bank_mask:0xf bound_ctrl:0\n\t"
      "s_nop 1\n\t"
      "v_add_u32_dpp v70, v70, v70 row_shr:4 row_mask:0xf bank_mask:0xf bound_ctrl:0\n\t"
      "v_cmp_ge_u32_e64 s[52:53], 64, v70\n\t"
      "v_cmp_lt_u32_e64 vcc, s41, v70\n\t"
      "s_and_b64 s[52:53], s[52:53], s[50:51]\n\t"
      "s_ff1_i32_b64 s45, s[52:53]\n\t"
      "s_and_b32 s46, s45, 48\n\t"
      "s_lshr_b64 s[52:53], vcc, s46\n\t"
      "s_and_b32 s52, s52, 0xff\n\t"
      "s_cbranch_scc0 IDLE_%=\n\t"
      "s_ff1_i32_b32 %[ob], s52\n\t"
      "s_add_u32 s47, s46, %[ob]\n\t"
      "v_readlane_b32 s54, v67, s47\n\t"
      "v_readlane_b32 %[on], v69, s47\n\t"
      "v_readlane_b32 s48, v70, s47\n\t"
      "v_readlane_b32 s59, v65, s47\n\t"
      "v_readlane_b32 %[olen], v64, s47\n\t"
      "s_sub_u32 s48, s48, %[on]\n\t"
      "s_sub_u32 %[oj], s41, s48\n\t"
      "s_mul_i32 s55, %[oj], s54\n\t"
      "s_lshl_b32 %[oslot], s42, 6\n\t"
      "s_add_u32 %[oslot], %[oslot], s48\n\t"
      "s_sub_u32 s59, s59, s55\n\t"
      "s_min_u32 %[ont], s54, s59\n\t"
      "s_lshl2_add_u32 s48, s55, s43\n\t"
      "s_lshl_b32 s48, s48, 2\n\t"
      "s_sub_u32 s66, 2048, s48\n\t"
      "s_lshl_b32 s59, %[ob], 11\n\t"
      "s_add_u32 s48, s48, s59\n\t"
      "s_add_u32 s64, s64, s48\n\t"
      "s_addc_u32 s65, s65, 0\n\t"
      "s_buffer_load_dword s53, s[64:67], 0x0\n\t"
      "s_buffer_load_dword s54, s[64:67], 0x10\n\t"
      "buffer_load_dword v15, v1, s[64:67], 0 offen\n\t"
      "s_cmp_eq_u32 %[ont], 0\n\t"
      "s_cbranch_scc1 FIN_%=\n\t"
      "s_lshl_b32 s59, %[ob], 18\n\t"
      "s_add_u32 s60, s60, s59\n\t"
      "s_addc_u32 s61, s61, 0\n\t"
      "s_add_u32 s82, s82, s59\n\t"
      "s_addc_u32 s83, s83, 0\n\t"
      "s_movk_i32 s50, 4096\n\t"
      "s_movk_i32 s51, 8192\n\t"
      "s_movk_i32 s52, 12288\n\t"
      "s_add_u32 m0, s49, 65536\n\t"
      "s_nop 0\n\t"
      "buffer_load_dwordx4 v56, s[60:63], 0 offen offset:0 lds\n\t"
      "buffer_load_dwordx4 v57, s[60:63], 0 offen offset:1024 lds\n\t"
      "buffer_load_dwordx4 v58, s[60:63], 0 offen offset:2048 lds\n\t"
      "buffer_load_dwordx4 v59, s[60:63], 0 offen offset:3072 lds\n\t"
      "s_add_u32 m0, s49, 81920\n\t"
      "s_nop 0\n\t"
      "buffer_load_dwordx4 v56, s[60:63], s50 offen offset:0 lds\n\t"
      "buffer_load_dwordx4 v57, s[60:63], s50 offen offset:1024 lds\n\t"
      "buffer_load_dwordx4 v58, s[60:63], s50 offen offset:2048 lds\n\t"
      "buffer_load_dwordx4 v59, s[60:63], s50 offen offset:3072 lds\n\t"
      "s_lshl2_add_u32 s48, s55, s43\n\t"
      "s_lshl_b32 s48, s48, 4\n\t"
      "s_sub_i32 s58, %[olen], s48\n\t"
      "v_lshlrev_b32 v40, 2, v1\n\t"
      "v_cmp_gt_i32_e64 s[68:69], s58, v40\n\t"
      "v_cmp_gt_u32_e64 vcc, %[ont], v32\n\t"
      "v_mov_b32 v41, 0x80000000\n\t"
      "s_and_b64 vcc, vcc, s[68:69]\n\t"
      "s_add_u32 s48, s55, %[ont]\n\t"
      "s_lshl_b32 s48, s48, 6\n\t"
      "s_sub_i32 s48, %[olen], s48\n\t"
      "s_add_i32 s48, s48, 64\n\t"
      "s_min_i32 s48, s48, 64\n\t"
      "s_lshl3_add_u32 s59, s42, %[ob]\n\t"
      "s_lshl_b32 s59, s59, 11\n\t"
      "s_add_u32 s84, s84, s59\n\t"
      "s_addc_u32 s85, s85, 0\n\t"
      "s_mov_b32 m0, s49\n\t"
      "v_mov_b32 v40, 0\n\t"
      "v_lshlrev_b32 v21, 2, v35\n\t"
      "v_sub_u32 v21, s48, v21\n\t"
      "s_add_u32 s44, s49, 81920\n\t"
      "s_waitcnt lgkmcnt(0)\n\t"
      "s_lshl_b32 s53, s53, 14\n\t"
      "s_lshl_b32 s54, s54, 14\n\t"
      "s_cmp_gt_i32 s58, 0\n\t"
      "s_cselect_b32 s53, s53, 0x80000000\n\t"
      "buffer_load_dwordx4 v7, s[36:39], s53 offen offset:0 nt lds\n\t"
      "buffer_load_dwordx4 v8, s[36:39], s53 offen offset:1024 nt lds\n\t"
      "buffer_load_dwordx4 v9, s[36:39], s53 offen offset:2048 nt lds\n\t"
      "buffer_load_dwordx4 v10, s[36:39], s53 offen offset:3072 nt lds\n\t"
      "s_add_u32 m0, s49, 131072\n\t"
      "s_nop 0\n\t"
      "buffer_load_dwordx4 v56, s[60:63], s51 offen offset:0 lds\n\t"
      "buffer_load_dwordx4 v57, s[60:63], s51 offen offset:1024 lds\n\t"
      "buffer_load_dwordx4 v58, s[60:63], s51 offen offset:2048 lds\n\t"
      "buffer_load_dwordx4 v59, s[60:63], s51 offen offset:3072 lds\n\t"
      "s_add_u32 m0, s49, 147456\n\t"
      "s_nop 0\n\t"
      "buffer_load_dwordx4 v56, s[60:63], s52 offen offset:0 lds\n\t"
      "buffer_load_dwordx4 v57, s[60:63], s52 offen offset:1024 lds\n\t"
      "buffer_load_dwordx4 v58, s[60:63], s52 offen offset:2048 lds\n\t"
      "buffer_load_dwordx4 v59, s[60:63], s52 offen offset:3072 lds\n\t"
      "s_cmp_gt_i32 s58, 64\n\t"
      "s_cselect_b32 s54, s54, 0x80000000\n\t"
      "s_cmp_gt_u32 %[ont], 1\n\t"
      "s_cselect_b32 s54, s54, 0x80000000\n\t"
      "s_add_u32 m0, s49, 16384\n\t"
      "s_nop 0\n\t"
      "buffer_load_dwordx4 v7, s[36:39], s54 offen offset:0 nt lds\n\t"
      "buffer_load_dwordx4 v8, s[36:39], s54 offen offset:1024 nt lds\n\t"
      "buffer_load_dwordx4 v9, s[36:39], s54 offen offset:2048 nt lds\n\t"
      "buffer_load_dwordx4 v10, s[36:39], s54 offen offset:3072 nt lds\n\t"
      "s_add_u32 m0, s44, 16384\n\t"
      "s_nop 0\n\t"
      "buffer_load_dwordx4 v11, s[72:75], s53 offen offset:0 nt lds\n\t"
      "buffer_load_dwordx4 v12, s[72:75], s53 offen offset:1024 nt lds\n\t"
      "buffer_load_dwordx4 v13, s[72:75], s53 offen offset:2048 nt lds\n\t"
      "buffer_load_dwordx4 v14, s[72:75], s53 offen offset:3072 nt lds\n\t"
      "global_load_dwordx2 v[22:23], v40, s[84:85] sc1\n\t"
      "s_waitcnt vmcnt(29)\n\t"
      "v_lshlrev_b32 v15, 14, v15\n\t"
      "v_cndmask_b32_e32 v15, v41, v15, vcc\n\t"
      "v_lshl_add_u32 v41, v32, 2, s43\n\t"
      "v_mul_u32_u24 v41, %[on], v41\n\t"
      "v_lshrrev_b32 v41, 6, v41\n\t"
      "v_cmp_eq_u32_e64 s[68:69], %[oj], v41\n\t"
      "s_sub_u32 s80, %[on], 4\n\t"
      "s_cmp_le_u32 s80, 12\n\t"
      "s_cselect_b32 s80, 0xffff, 0\n\t"
      "s_and_b32 s80, s80, s68\n\t"
      "s_cmp_lt_i32 s48, 64\n\t"
      "s_cselect_b64 s[68:69], -1, 0\n\t"
      "s_cmp_eq_u32 %[ont], 1\n\t"
      "s_cselect_b64 vcc, s[68:69], 0\n\t"
      "s_waitcnt vmcnt(25)\n\t"
      "ds_read_b128 v[64:67], v60 offset:0\n\t"
      "ds_read_b128 v[68:71], v61 offset:0\n\t"
      "ds_read_b128 v[72:75], v60 offset:1024\n\t"
      "ds_read_b128 v[76:79], v61 offset:1024\n\t"
      "v_readlane_b32 s51, v15, 1\n\t"
      "v_readlane_b32 s52, v15, 2\n\t"
      "v_readlane_b32 s53, v15, 3\n\t"
      "v_readlane_b32 s54, v15, 4\n\t"
      "v_readlane_b32 s46, v15, 3\n\t"
      "v_readlane_b32 s45, v15, 4\n\t"
      "s_movk_i32 s55, 4096\n\t"
      "s_movk_i32 s56, 8192\n\t"
      "s_movk_i32 s57, 12288\n\t"
      "s_mov_b32 s40, 0x3e0293ee\n\t"
      "s_mov_b32 s58, 0xffff0000\n\t"
      "s_mov_b32 s43, 65536\n\t"
      "s_mov_b32 s42, 0x4000\n\t"
      "s_mov_b32 s41, 0x8000\n\t"
      "s_mov_b32 s59, 0x4000\n\t"
      "s_mov_b32 s47, 3\n\t"
      "s_add_u32 s48, %[ont], 2\n\t"
      "s_waitcnt vmcnt(21)\n\t"
      "ds_read_b128 v[80:83], v60 offset:16384\n\t"
      "ds_read_b128 v[84:87], v61 offset:16384\n\t"
      "ds_read_b128 v[88:91], v60 offset:17408\n\t"
      "ds_read_b128 v[92:95], v61 offset:17408\n\t"
      "s_waitcnt lgkmcnt(4)\n\t"
      "v_lshlrev_b32 v32, 16, v64\n\t"
      "v_and_b32 v33, s58, v64\n\t"
      "v_lshlrev_b32 v34, 16, v65\n\t"
      "v_and_b32 v35, s58, v65\n\t"
      "v_lshlrev_b32 v36, 16, v66\n\t"
      "v_and_b32 v37, s58, v66\n\t"
      "v_lshlrev_b32 v38, 16, v67\n\t"
      "v_and_b32 v39, s58, v67\n\t"
      "v_pk_mul_f32 v[32:33], v[32:33], s[40:41] op_sel_hi:[1,0]\n\t"
      "v_pk_mul_f32 v[34:35], v[34:35], s[40:41] op_sel_hi:[1,0]\n\t"
      "v_pk_mul_f32 v[36:37], v[36:37], s[40:41] op_sel_hi:[1,0]\n\t"
      "v_pk_mul_f32 v[38:39], v[38:39], s[40:41] op_sel_hi:[1,0]\n\t"
      "v_cvt_pk_bf16_f32 v64, v32, v33\n\t"
      "v_cvt_pk_bf16_f32 v65, v34, v35\n\t"
      "v_cvt_pk_bf16_f32 v66, v36, v37\n\t"
      "v_cvt_pk_bf16_f32 v67, v38, v39\n\t"
      "v_accvgpr_write_b32 a0, v64\n\t"
      "v_accvgpr_write_b32 a1, v65\n\t"
      "v_accvgpr_write_b32 a2, v66\n\t"
      "v_accvgpr_write_b32 a3, v67\n\t"
      "v_lshlrev_b32 v32, 16, v68\n\t"
      "v_and_b32 v33, s58, v68\n\t"
      "v_lshlrev_b32 v34, 16, v69\n\t"
      "v_and_b32 v35, s58, v69\n\t"
      "v_lshlrev_b32 v36, 16, v70\n\t"
      "v_and_b32 v37, s58, v70\n\t"
      "v_lshlrev_b32 v38, 16, v71\n\t"
      "v_and_b32 v39, s58, v71\n\t"
      "v_pk_mul_f32 v[32:33], v[32:33], s[40:41] op_sel_hi:[1,0]\n\t"
      "v_pk_mul_f32 v[34:35], v[34:35], s[40:41] op_sel_hi:[1,0]\n\t"
      "v_pk_mul_f32 v[36:37], v[36:37], s[40:41] op_sel_hi:[1,0]\n\t"
      "v_pk_mul_f32 v[38:39], v[38:39], s[40:41] op_sel_hi:[1,0]\n\t"
      "v_cvt_pk_bf16_f32 v68, v32, v33\n\t"
      "v_cvt_pk_bf16_f32 v69, v34, v35\n\t"
      "v_cvt_pk_bf16_f32 v70, v36, v37\n\t"
      "v_cvt_pk_bf16_f32 v71, v38, v39\n\t"
      "v_accvgpr_write_b32 a4, v68\n\t"
      "v_accvgpr_write_b32 a5, v69\n\t"
      "v_accvgpr_write_b32 a6, v70\n\t"
      "v_accvgpr_write_b32 a7, v71\n\t"
      "v_lshlrev_b32 v32, 16, v72\n\t"
      "v_and_b32 v33, s58, v72\n\t"
      "v_lshlrev_b32 v34, 16, v73\n\t"
      "v_and_b32 v35, s58, v73\n\t"
      "v_lshlrev_b32 v36, 16, v74\n\t"
      "v_and_b32 v37, s58, v74\n\t"
      "v_lshlrev_b32 v38, 16, v75\n\t"
      "v_and_b32 v39, s58, v75\n\t"
      "v_pk_mul_f32 v[32:33], v[32:33], s[40:41] op_sel_hi:[1,0]\n\t"
      "v_pk_mul_f32 v[34:35], v[34:35], s[40:41] op_sel_hi:[1,0]\n\t"
      "v_pk_mul_f32 v[36:37], v[36:37], s[40:41] op_sel_hi:[1,0]\n\t"
      "v_pk_mul_f32 v[38:39], v[38:39], s[40:41] op_sel_hi:[1,0]\n\t"
      "v_cvt_pk_bf16_f32 v72, v32, v33\n\t"
      "v_cvt_pk_bf16_f32 v73, v34, v35\n\t"
      "v_cvt_pk_bf16_f32 v74, v36, v37\n\t"
      "v_cvt_pk_bf16_f32 v75, v38, v39\n\t"
      "v_accvgpr_write_b32 a8, v72\n\t"
      "v_accvgpr_write_b32 a9, v73\n\t"
      "v_accvgpr_write_b32 a10, v74\n\t"
      "v_accvgpr_write_b32 a11, v75\n\t"
      "v_lshlrev_b32 v32, 16, v76\n\t"
      "v_and_b32 v33, s58, v76\n\t"
      "v_lshlrev_b32 v34, 16, v77\n\t"
      "v_and_b32 v35, s58, v77\n\t"
      "v_lshlrev_b32 v36, 16, v78\n\t"
      "v_and_b32 v37, s58, v78\n\t"
      "v_lshlrev_b32 v38, 16, v79\n\t"
      "v_and_b32 v39, s58, v79\n\t"
      "v_pk_mul_f32 v[32:33], v[32:33], s[40:41] op_sel_hi:[1,0]\n\t"
      "v_pk_mul_f32 v[34:35], v[34:35], s[40:41] op_sel_hi:[1,0]\n\t"
      "v_pk_mul_f32 v[36:37], v[36:37], s[40:41] op_sel_hi:[1,0]\n\t"
      "v_pk_mul_f32 v[38:39], v[38:39], s[40:41] op_sel_hi:[1,0]\n\t"
      "v_cvt_pk_bf16_f32 v76, v32, v33\n\t"
      "v_cvt_pk_bf16_f32 v77, v34, v35\n\t"
      "v_cvt_pk_bf16_f32 v78, v36, v37\n\t"
      "v_cvt_pk_bf16_f32 v79, v38, v39\n\t"
      "v_accvgpr_write_b32 a12, v76\n\t"
      "v_accvgpr_write_b32 a13, v77\n\t"
      "v_accvgpr_write_b32 a14, v78\n\t"
      "v_accvgpr_write_b32 a15, v79\n\t"
      "s_waitcnt lgkmcnt(0)\n\t"
      "v_lshlrev_b32 v32, 16, v80\n\t"
      "v_and_b32 v33, s58, v80\n\t"
      "v_lshlrev_b32 v34, 16, v81\n\t"
      "v_and_b32 v35, s58, v81\n\t"
      "v_lshlrev_b32 v36, 16, v82\n\t"
      "v_and_b32 v37, s58, v82\n\t"
      "v_lshlrev_b32 v38, 16, v83\n\t"
      "v_and_b32 v39, s58, v83\n\t"
      "v_pk_mul_f32 v[32:33], v[32:33], s[40:41] op_sel_hi:[1,0]\n\t"
      "v_pk_mul_f32 v[34:35], v[34:35], s[40:41] op_sel_hi:[1,0]\n\t"
      "v_pk_mul_f32 v[36:37], v[36:37], s[40:41] op_sel_hi:[1,0]\n\t"
      "v_pk_mul_f32 v[38:39], v[38:39], s[40:41] op_sel_hi:[1,0]\n\t"
      "v_cvt_pk_bf16_f32 v80, v32, v33\n\t"
      "v_cvt_pk_bf16_f32 v81, v34, v35\n\t"
      "v_cvt_pk_bf16_f32 v82, v36, v37\n\t"
      "v_cvt_pk_bf16_f32 v83, v38, v39\n\t"
      "v_accvgpr_write_b32 a16, v80\n\t"
      "v_accvgpr_write_b32 a17, v81\n\t"
      "v_accvgpr_write_b32 a18, v82\n\t"
      "v_accvgpr_write_b32 a19, v83\n\t"
      "v_lshlrev_b32 v32, 16, v84\n\t"
      "v_and_b32 v33, s58, v84\n\t"
      "v_lshlrev_b32 v34, 16, v85\n\t"
      "v_and_b32 v35, s58, v85\n\t"
      "v_lshlrev_b32 v36, 16, v86\n\t"
      "v_and_b32 v37, s58, v86\n\t"
      "v_lshlrev_b32 v38, 16, v87\n\t"
      "v_and_b32 v39, s58, v87\n\t"
      "v_pk_mul_f32 v[32:33], v[32:33], s[40:41] op_sel_hi:[1,0]\n\t"
      "v_pk_mul_f32 v[34:35], v[34:35], s[40:41] op_sel_hi:[1,0]\n\t"
      "v_pk_mul_f32 v[36:37], v[36:37], s[40:41] op_sel_hi:[1,0]\n\t"
      "v_pk_mul_f32 v[38:39], v[38:39], s[40:41] op_sel_hi:[1,0]\n\t"
      "v_cvt_pk_bf16_f32 v84, v32, v33\n\t"
      "v_cvt_pk_bf16_f32 v85, v34, v35\n\t"
      "v_cvt_pk_bf16_f32 v86, v36, v37\n\t"
      "v_cvt_pk_bf16_f32 v87, v38, v39\n\t"
      "v_accvgpr_write_b32 a20, v84\n\t"
      "v_accvgpr_write_b32 a21, v85\n\t"
      "v_accvgpr_write_b32 a22, v86\n\t"
      "v_accvgpr_write_b32 a23, v87\n\t"
      "v_lshlrev_b32 v32, 16, v88\n\t"
      "v_and_b32 v33, s58, v88\n\t"
      "v_lshlrev_b32 v34, 16, v89\n\t"
      "v_and_b32 v35, s58, v89\n\t"
      "v_lshlrev_b32 v36, 16, v90\n\t"
      "v_and_b32 v37, s58, v90\n\t"
      "v_lshlrev_b32 v38, 16, v91\n\t"
      "v_and_b32 v39, s58, v91\n\t"
      "v_pk_mul_f32 v[32:33], v[32:33], s[40:41] op_sel_hi:[1,0]\n\t"
      "v_pk_mul_f32 v[34:35], v[34:35], s[40:41] op_sel_hi:[1,0]\n\t"
      "v_pk_mul_f32 v[36:37], v[36:37], s[40:41] op_sel_hi:[1,0]\n\t"
      "v_pk_mul_f32 v[38:39], v[38:39], s[40:41] op_sel_hi:[1,0]\n\t"
      "v_cvt_pk_bf16_f32 v88, v32, v33\n\t"
      "v_cvt_pk_bf16_f32 v89, v34, v35\n\t"
      "v_cvt_pk_bf16_f32 v90, v36, v37\n\t"
      "v_cvt_pk_bf16_f32 v91, v38, v39\n\t"
      "v_accvgpr_write_b32 a24, v88\n\t"
      "v_accvgpr_write_b32 a25, v89\n\t"
      "v_accvgpr_write_b32 a26, v90\n\t"
      "v_accvgpr_write_b32 a27, v91\n\t"
      "v_lshlrev_b32 v32, 16, v92\n\t"
      "v_and_b32 v33, s58, v92\n\t"
      "v_lshlrev_b32 v34, 16, v93\n\t"
      "v_and_b32 v35, s58, v93\n\t"
      "v_lshlrev_b32 v36, 16, v94\n\t"
      "v_and_b32 v37, s58, v94\n\t"
      "v_lshlrev_b32 v38, 16, v95\n\t"
      "v_and_b32 v39, s58, v95\n\t"
      "v_pk_mul_f32 v[32:33], v[32:33], s[40:41] op_sel_hi:[1,0]\n\t"
      "v_pk_mul_f32 v[34:35], v[34:35], s[40:41] op_sel_hi:[1,0]\n\t"
      "v_pk_mul_f32 v[36:37], v[36:37], s[40:41] op_sel_hi:[1,0]\n\t"
      "v_pk_mul_f32 v[38:39], v[38:39], s[40:41] op_sel_hi:[1,0]\n\t"
      "v_cvt_pk_bf16_f32 v92, v32, v33\n\t"
      "v_cvt_pk_bf16_f32 v93, v34, v35\n\t"
      "v_cvt_pk_bf16_f32 v94, v36, v37\n\t"
      "v_cvt_pk_bf16_f32 v95, v38, v39\n\t"
      "v_accvgpr_write_b32 a28, v92\n\t"
      "v_accvgpr_write_b32 a29, v93\n\t"
      "v_accvgpr_write_b32 a30, v94\n\t"
      "v_accvgpr_write_b32 a31, v95\n\t"
      "s_waitcnt vmcnt(17)\n\t"
      "s_barrier\n\t"
      "ds_read_b128 a[64:67], v2 offset:0\n\t"
      "ds_read_b128 a[68:71], v3 offset:0\n\t"
      "ds_read_b128 a[72:75], v2 offset:1024\n\t"
      "ds_read_b128 a[76:79], v3 offset:1024\n\t"
      "ds_read_b128 a[80:83], v2 offset:4096\n\t"
      "ds_read_b128 a[84:87], v3 offset:4096\n\t"
      "ds_read_b128 a[88:91], v2 offset:5120\n\t"
      "ds_read_b128 a[92:95], v3 offset:5120\n\t"
      "s_waitcnt vmcnt(9)\n\t"
      "ds_read_b128 v[96:99], v62 offset:0\n\t"
      "ds_read_b128 v[100:103], v63 offset:0\n\t"
      "ds_read_b128 v[104:107], v62 offset:1024\n\t"
      "ds_read_b128 v[108:111], v63 offset:1024\n\t"
      "ds_read_b128 v[112:115], v62 offset:16384\n\t"
      "ds_read_b128 v[116:119], v63 offset:16384\n\t"
      "ds_read_b128 v[120:123], v62 offset:17408\n\t"
      "ds_read_b128 v[124:127], v63 offset:17408\n\t"
      "ds_read_b128 a[96:99], v2 offset:8192\n\t"
      "ds_read_b128 a[100:103], v3 offset:8192\n\t"
      "ds_read_b128 a[104:107], v2 offset:9216\n\t"
      "ds_read_b128 a[108:111], v3 offset:9216\n\t"
      "ds_read_b128 a[112:115], v2 offset:12288\n\t"
      "ds_read_b128 a[116:119], v3 offset:12288\n\t"
      "ds_read_b128 a[120:123], v2 offset:13312\n\t"
      "ds_read_b128 a[124:127], v3 offset:13312\n\t"
      "s_waitcnt lgkmcnt(12)\n\t"
      "v_lshlrev_b32 v32, 16, v96\n\t"
      "v_and_b32 v33, s58, v96\n\t"
      "s_add_u32 m0, s49, 32768\n\t"
      "s_nop 0\n\t"
      "buffer_load_dwordx4 v7, s[36:39], s52 offen offset:0 nt lds\n\t"
      "v_lshlrev_b32 v34, 16, v97\n\t"
      "v_and_b32 v35, s58, v97\n\t"
      "v_lshlrev_b32 v36, 16, v98\n\t"
      "v_and_b32 v37, s58, v98\n\t"
      "v_lshlrev_b32 v38, 16, v99\n\t"
      "v_and_b32 v39, s58, v99\n\t"
      "v_pk_mul_f32 v[32:33], v[32:33], s[40:41] op_sel_hi:[1,0]\n\t"
      "v_pk_mul_f32 v[34:35], v[34:35], s[40:41] op_sel_hi:[1,0]\n\t"
      "v_pk_mul_f32 v[36:37], v[36:37], s[40:41] op_sel_hi:[1,0]\n\t"
      "v_pk_mul_f32 v[38:39], v[38:39], s[40:41] op_sel_hi:[1,0]\n\t"
      "v_cvt_pk_bf16_f32 v96, v32, v33\n\t"
      "v_cvt_pk_bf16_f32 v97, v34, v35\n\t"
      "v_cvt_pk_bf16_f32 v98, v36, v37\n\t"
      "v_cvt_pk_bf16_f32 v99, v38, v39\n\t"
      "v_accvgpr_write_b32 a32, v96\n\t"
      "v_accvgpr_write_b32 a33, v97\n\t"
      "buffer_load_dwordx4 v8, s[36:39], s52 offen offset:1024 nt lds\n\t"
      "v_accvgpr_write_b32 a34, v98\n\t"
      "v_accvgpr_write_b32 a35, v99\n\t"
      "v_lshlrev_b32 v32, 16, v100\n\t"
      "v_and_b32 v33, s58, v100\n\t"
      "v_lshlrev_b32 v34, 16, v101\n\t"
      "v_and_b32 v35, s58, v101\n\t"
      "v_lshlrev_b32 v36, 16, v102\n\t"
      "v_and_b32 v37, s58, v102\n\t"
      "v_lshlrev_b32 v38, 16, v103\n\t"
      "v_and_b32 v39, s58, v103\n\t"
      "v_pk_mul_f32 v[32:33], v[32:33], s[40:41] op_sel_hi:[1,0]\n\t"
      "v_pk_mul_f32 v[34:35], v[34:35], s[40:41] op_sel_hi:[1,0]\n\t"
      "v_pk_mul_f32 v[36:37], v[36:37], s[40:41] op_sel_hi:[1,0]\n\t"
      "v_pk_mul_f32 v[38:39], v[38:39], s[40:41] op_sel_hi:[1,0]\n\t"
      "v_cvt_pk_bf16_f32 v100, v32, v33\n\t"
      "v_cvt_pk_bf16_f32 v101, v34, v35\n\t"
      "buffer_load_dwordx4 v9, s[36:39], s52 offen offset:2048 nt lds\n\t"
      "v_cvt_pk_bf16_f32 v102, v36, v37\n\t"
      "v_cvt_pk_bf16_f32 v103, v38, v39\n\t"
      "v_accvgpr_write_b32 a36, v100\n\t"
      "v_accvgpr_write_b32 a37, v101\n\t"
      "v_accvgpr_write_b32 a38, v102\n\t"
      "v_accvgpr_write_b32 a39, v103\n\t"
      "v_lshlrev_b32 v32, 16, v104\n\t"
      "v_and_b32 v33, s58, v104\n\t"
      "v_lshlrev_b32 v34, 16, v105\n\t"
      "v_and_b32 v35, s58, v105\n\t"
      "v_lshlrev_b32 v36, 16, v106\n\t"
      "v_and_b32 v37, s58, v106\n\t"
      "v_lshlrev_b32 v38, 16, v107\n\t"
      "v_and_b32 v39, s58, v107\n\t"
      "v_pk_mul_f32 v[32:33], v[32:33], s[40:41] op_sel_hi:[1,0]\n\t"
      "v_pk_mul_f32 v[34:35], v[34:35], s[40:41] op_sel_hi:[1,0]\n\t"
      "buffer_load_dwordx4 v10, s[36:39], s52 offen offset:3072 nt lds\n\t"
      "v_pk_mul_f32 v[36:37], v[36:37], s[40:41] op_sel_hi:[1,0]\n\t"
      "v_pk_mul_f32 v[38:39], v[38:39], s[40:41] op_sel_hi:[1,0]\n\t"
      "v_cvt_pk_bf16_f32 v104, v32, v33\n\t"
      "v_cvt_pk_bf16_f32 v105, v34, v35\n\t"
      "v_cvt_pk_bf16_f32 v106, v36, v37\n\t"
      "v_cvt_pk_bf16_f32 v107, v38, v39\n\t"
      "v_accvgpr_write_b32 a40, v104\n\t"
      "v_accvgpr_write_b32 a41, v105\n\t"
      "v_accvgpr_write_b32 a42, v106\n\t"
      "v_accvgpr_write_b32 a43, v107\n\t"
      "v_lshlrev_b32 v32, 16, v108\n\t"
      "v_and_b32 v33, s58, v108\n\t"
      "v_lshlrev_b32 v34, 16, v109\n\t"
      "v_and_b32 v35, s58, v109\n\t"
      "v_lshlrev_b32 v36, 16, v110\n\t"
      "v_and_b32 v37, s58, v110\n\t"
      "v_lshlrev_b32 v38, 16, v111\n\t"
      "v_and_b32 v39, s58, v111\n\t"
      "v_pk_mul_f32 v[32:33], v[32:33], s[40:41] op_sel_hi:[1,0]\n\t"
      "v_pk_mul_f32 v[34:35], v[34:35], s[40:41] op_sel_hi:[1,0]\n\t"
      "v_pk_mul_f32 v[36:37], v[36:37], s[40:41] op_sel_hi:[1,0]\n\t"
      "v_pk_mul_f32 v[38:39], v[38:39], s[40:41] op_sel_hi:[1,0]\n\t"
      "v_cvt_pk_bf16_f32 v108, v32, v33\n\t"
      "v_cvt_pk_bf16_f32 v109, v34, v35\n\t"
      "v_cvt_pk_bf16_f32 v110, v36, v37\n\t"
      "v_cvt_pk_bf16_f32 v111, v38, v39\n\t"
      "v_accvgpr_write_b32 a44, v108\n\t"
      "v_accvgpr_write_b32 a45, v109\n\t"
      "v_accvgpr_write_b32 a46, v110\n\t"
      "v_accvgpr_write_b32 a47, v111\n\t"
      "s_waitcnt lgkmcnt(8)\n\t"
      "v_lshlrev_b32 v32, 16, v112\n\t"
      "v_and_b32 v33, s58, v112\n\t"
      "v_lshlrev_b32 v34, 16, v113\n\t"
      "v_and_b32 v35, s58, v113\n\t"
      "v_lshlrev_b32 v36, 16, v114\n\t"
      "v_and_b32 v37, s58, v114\n\t"
      "v_lshlrev_b32 v38, 16, v115\n\t"
      "v_and_b32 v39, s58, v115\n\t"
      "v_pk_mul_f32 v[32:33], v[32:33], s[40:41] op_sel_hi:[1,0]\n\t"
      "v_pk_mul_f32 v[34:35], v[34:35], s[40:41] op_sel_hi:[1,0]\n\t"
      "v_pk_mul_f32 v[36:37], v[36:37], s[40:41] op_sel_hi:[1,0]\n\t"
      "v_pk_mul_f32 v[38:39], v[38:39], s[40:41] op_sel_hi:[1,0]\n\t"
      "v_cvt_pk_bf16_f32 v112, v32, v33\n\t"
      "v_cvt_pk_bf16_f32 v113, v34, v35\n\t"
      "v_cvt_pk_bf16_f32 v114, v36, v37\n\t"
      "v_cvt_pk_bf16_f32 v115, v38, v39\n\t"
      "v_accvgpr_write_b32 a48, v112\n\t"
      "v_accvgpr_write_b32 a49, v113\n\t"
      "v_accvgpr_write_b32 a50, v114\n\t"
      "v_accvgpr_write_b32 a51, v115\n\t"
      "v_lshlrev_b32 v32, 16, v116\n\t"
      "v_and_b32 v33, s58, v116\n\t"
      "v_lshlrev_b32 v34, 16, v117\n\t"
      "v_and_b32 v35, s58, v117\n\t"
      "v_lshlrev_b32 v36, 16, v118\n\t"
      "v_and_b32 v37, s58, v118\n\t"
      "v_lshlrev_b32 v38, 16, v119\n\t"
      "v_and_b32 v39, s58, v119\n\t"
      "v_pk_mul_f32 v[32:33], v[32:33], s[40:41] op_sel_hi:[1,0]\n\t"
      "v_pk_mul_f32 v[34:35], v[34:35], s[40:41] op_sel_hi:[1,0]\n\t"
      "v_pk_mul_f32 v[36:37], v[36:37], s[40:41] op_sel_hi:[1,0]\n\t"
      "v_pk_mul_f32 v[38:39], v[38:39], s[40:41] op_sel_hi:[1,0]\n\t"
      "v_cvt_pk_bf16_f32 v116, v32, v33\n\t"
      "v_cvt_pk_bf16_f32 v117, v34, v35\n\t"
      "v_cvt_pk_bf16_f32 v118, v36, v37\n\t"
      "v_cvt_pk_bf16_f32 v119, v38, v39\n\t"
      "v_accvgpr_write_b32 a52, v116\n\t"
      "v_accvgpr_write_b32 a53, v117\n\t"
      "v_accvgpr_write_b32 a54, v118\n\t"
      "v_accvgpr_write_b32 a55, v119\n\t"
      "v_lshlrev_b32 v32, 16, v120\n\t"
      "v_and_b32 v33, s58, v120\n\t"
      "v_lshlrev_b32 v34, 16, v121\n\t"
      "v_and_b32 v35, s58, v121\n\t"
      "v_lshlrev_b32 v36, 16, v122\n\t"
      "v_and_b32 v37, s58, v122\n\t"
      "v_lshlrev_b32 v38, 16, v123\n\t"
      "v_and_b32 v39, s58, v123\n\t"
      "v_pk_mul_f32 v[32:33], v[32:33], s[40:41] op_sel_hi:[1,0]\n\t"
      "v_pk_mul_f32 v[34:35], v[34:35], s[40:41] op_sel_hi:[1,0]\n\t"
      "v_pk_mul_f32 v[36:37], v[36:37], s[40:41] op_sel_hi:[1,0]\n\t"
      "v_pk_mul_f32 v[38:39], v[38:39], s[40:41] op_sel_hi:[1,0]\n\t"
      "v_cvt_pk_bf16_f32 v120, v32, v33\n\t"
      "v_cvt_pk_bf16_f32 v121, v34, v35\n\t"
      "v_cvt_pk_bf16_f32 v122, v36, v37\n\t"
      "v_cvt_pk_bf16_f32 v123, v38, v39\n\t"
      "v_accvgpr_write_b32 a56, v120\n\t"
      "v_accvgpr_write_b32 a57, v121\n\t"
      "v_accvgpr_write_b32 a58, v122\n\t"
      "v_accvgpr_write_b32 a59, v123\n\t"
      "v_lshlrev_b32 v32, 16, v124\n\t"
      "v_and_b32 v33, s58, v124\n\t"
      "v_lshlrev_b32 v34, 16, v125\n\t"
      "v_and_b32 v35, s58, v125\n\t"
      "v_lshlrev_b32 v36, 16, v126\n\t"
      "v_and_b32 v37, s58, v126\n\t"
      "v_lshlrev_b32 v38, 16, v127\n\t"
      "v_and_b32 v39, s58, v127\n\t"
      "v_pk_mul_f32 v[32:33], v[32:33], s[40:41] op_sel_hi:[1,0]\n\t"
      "v_pk_mul_f32 v[34:35], v[34:35], s[40:41] op_sel_hi:[1,0]\n\t"
      "v_pk_mul_f32 v[36:37], v[36:37], s[40:41] op_sel_hi:[1,0]\n\t"
      "v_pk_mul_f32 v[38:39], v[38:39], s[40:41] op_sel_hi:[1,0]\n\t"
      "v_cvt_pk_bf16_f32 v124, v32, v33\n\t"
      "v_cvt_pk_bf16_f32 v125, v34, v35\n\t"
      "v_cvt_pk_bf16_f32 v126, v36, v37\n\t"
      "v_cvt_pk_bf16_f32 v127, v38, v39\n\t"
      "v_accvgpr_write_b32 a60, v124\n\t"
      "v_accvgpr_write_b32 a61, v125\n\t"
      "v_accvgpr_write_b32 a62, v126\n\t"
      "v_accvgpr_write_b32 a63, v127\n\t"
      "v_mfma_f32_16x16x32_bf16 v[64:67], a[64:67], a[0:3], 0\n\t"
      "v_mfma_f32_16x16x32_bf16 v[64:67], a[68:71], a[4:7], v[64:67]\n\t"
      "v_mfma_f32_16x16x32_bf16 v[64:67], a[72:75], a[8:11], v[64:67]\n\t"
      "v_mfma_f32_16x16x32_bf16 v[64:67], a[76:79], a[12:15], v[64:67]\n\t"
      "v_mfma_f32_16x16x32_bf16 v[68:71], a[80:83], a[0:3], 0\n\t"
      "v_mfma_f32_16x16x32_bf16 v[68:71], a[84:87], a[4:7], v[68:71]\n\t"
      "v_mfma_f32_16x16x32_bf16 v[68:71], a[88:91], a[8:11], v[68:71]\n\t"
      "v_mfma_f32_16x16x32_bf16 v[68:71], a[92:95], a[12:15], v[68:71]\n\t"
      "v_mfma_f32_16x16x32_bf16 v[72:75], a[64:67], a[16:19], 0\n\t"
      "v_mfma_f32_16x16x32_bf16 v[72:75], a[68:71], a[20:23], v[72:75]\n\t"
      "v_mfma_f32_16x16x32_bf16 v[72:75], a[72:75], a[24:27], v[72:75]\n\t"
      "v_mfma_f32_16x16x32_bf16 v[72:75], a[76:79], a[28:31], v[72:75]\n\t"
      "v_mfma_f32_16x16x32_bf16 v[76:79], a[80:83], a[16:19], 0\n\t"
      "v_mfma_f32_16x16x32_bf16 v[76:79], a[84:87], a[20:23], v[76:79]\n\t"
      "v_mfma_f32_16x16x32_bf16 v[76:79], a[88:91], a[24:27], v[76:79]\n\t"
      "v_mfma_f32_16x16x32_bf16 v[76:79], a[92:95], a[28:31], v[76:79]\n\t"
      "v_mfma_f32_16x16x32_bf16 v[80:83], a[64:67], a[32:35], 0\n\t"
      "v_mfma_f32_16x16x32_bf16 v[80:83], a[68:71], a[36:39], v[80:83]\n\t"
      "v_mfma_f32_16x16x32_bf16 v[80:83], a[72:75], a[40:43], v[80:83]\n\t"
      "v_mfma_f32_16x16x32_bf16 v[80:83], a[76:79], a[44:47], v[80:83]\n\t"
      "v_mfma_f32_16x16x32_bf16 v[84:87], a[80:83], a[32:35], 0\n\t"
      "v_mfma_f32_16x16x32_bf16 v[84:87], a[84:87], a[36:39], v[84:87]\n\t"
      "v_mfma_f32_16x16x32_bf16 v[84:87], a[88:91], a[40:43], v[84:87]\n\t"
      "v_mfma_f32_16x16x32_bf16 v[84:87], a[92:95], a[44:47], v[84:87]\n\t"
      "v_mfma_f32_16x16x32_bf16 v[88:91], a[64:67], a[48:51], 0\n\t"
      "v_mfma_f32_16x16x32_bf16 v[88:91], a[68:71], a[52:55], v[88:91]\n\t"
      "v_mfma_f32_16x16x32_bf16 v[88:91], a[72:75], a[56:59], v[88:91]\n\t"
      "v_mfma_f32_16x16x32_bf16 v[88:91], a[76:79], a[60:63], v[88:91]\n\t"
      "v_mfma_f32_16x16x32_bf16 v[92:95], a[80:83], a[48:51], 0\n\t"
      "v_mfma_f32_16x16x32_bf16 v[92:95], a[84:87], a[52:55], v[92:95]\n\t"
      "v_mfma_f32_16x16x32_bf16 v[92:95], a[88:91], a[56:59], v[92:95]\n\t"
      "s_waitcnt lgkmcnt(0)\n\t"
      "v_mfma_f32_16x16x32_bf16 v[92:95], a[92:95], a[60:63], v[92:95]\n\t"
      "s_cbranch_vccnz MKF0_%=\n\t"
      "MBF0_%=:\n\t"
      "v_max3_f32 v24, v64, v65, v66\n\t"
      "v_max3_f32 v24, v24, v67, v68\n\t"
      "v_max3_f32 v24, v24, v69, v70\n\t"
      "v_max_f32 v24, v24, v71\n\t"
      "v_max3_f32 v26, v80, v81, v82\n\t"
      "v_max3_f32 v26, v26, v83, v84\n\t"
      "v_max3_f32 v26, v26, v85, v86\n\t"
      "v_max_f32 v26, v26, v87\n\t"
      "v_max3_f32 v25, v72, v73, v74\n\t"
      "v_max3_f32 v25, v25, v75, v76\n\t"
      "v_max3_f32 v25, v25, v77, v78\n\t"
      "v_max_f32 v25, v25, v79\n\t"
      "v_permlane32_swap_b32 v24, v26\n\t"
      "v_max3_f32 v27, v88, v89, v90\n\t"
      "v_max3_f32 v27, v27, v91, v92\n\t"
      "v_max3_f32 v27, v27, v93, v94\n\t"
      "v_max_f32 v27, v27, v95\n\t"
      "v_max_f32 v24, v24, v26\n\t"
      "s_nop 0\n\t"
      "v_permlane32_swap_b32 v25, v27\n\t"
      "v_max_f32 v25, v25, v27\n\t"
      "s_nop 1\n\t"
      "v_permlane16_swap_b32 v24, v25\n\t"
      "v_min_f32_e64 v16, -v24, -v25\n\t"
      "v_mov_b32 v32, v16\n\t"
      "v_mov_b32 v40, v16\n\t"
      "s_nop 1\n\t"
      "v_permlane32_swap_b32 v32, v40\n\t"
      "v_mov_b32 v36, v32\n\t"
      "v_mov_b32 v44, v40\n\t"
      "s_nop 0\n\t"
      "v_permlane16_swap_b32 v32, v36\n\t"
      "v_permlane16_swap_b32 v40, v44\n\t"
      "v_mov_b32 v33, v32\n\t"
      "v_mov_b64 v[34:35], v[32:33]\n\t"
      "v_mov_b32 v37, v36\n\t"
      "v_mov_b64 v[38:39], v[36:37]\n\t"
      "v_mfma_f32_16x16x32_bf16 v[96:99], a[96:99], a[0:3], v[32:35]\n\t"
      "v_mov_b32 v41, v40\n\t"
      "v_mov_b64 v[42:43], v[40:41]\n\t"
      "v_mov_b32 v45, v44\n\t"
      "v_mfma_f32_16x16x32_bf16 v[96:99], a[100:103], a[4:7], v[96:99]\n\t"
      "v_mov_b64 v[46:47], v[44:45]\n\t"
      "v_add_f32 v64, v64, v32\n\t"
      "v_mfma_f32_16x16x32_bf16 v[96:99], a[104:107], a[8:11], v[96:99]\n\t"
      "v_exp_f32 v64, v64\n\t"
      "v_add_f32 v65, v65, v32\n\t"
      "v_mfma_f32_16x16x32_bf16 v[96:99], a[108:111], a[12:15], v[96:99]\n\t"
      "s_add_u32 m0, s44, 32768\n\t"
      "s_nop 0\n\t"
      "buffer_load_dwordx4 v11, s[72:75], s51 offen offset:0 nt lds\n\t"
      "v_exp_f32 v65, v65\n\t"
      "v_add_f32 v66, v66, v32\n\t"
      "v_mfma_f32_16x16x32_bf16 v[100:103], a[112:115], a[0:3], v[32:35]\n\t"
      "v_exp_f32 v66, v66\n\t"
      "v_add_f32 v67, v67, v32\n\t"
      "v_cvt_pk_bf16_f32 v64, v64, v65\n\t"
      "v_mfma_f32_16x16x32_bf16 v[100:103], a[116:119], a[4:7], v[100:103]\n\t"
      "v_exp_f32 v67, v67\n\t"
      "v_add_f32 v68, v68, v32\n\t"
      "v_mfma_f32_16x16x32_bf16 v[100:103], a[120:123], a[8:11], v[100:103]\n\t"
      "v_exp_f32 v68, v68\n\t"
      "v_add_f32 v69, v69, v32\n\t"
      "v_mfma_f32_16x16x32_bf16 v[100:103], a[124:127], a[12:15], v[100:103]\n\t"
      "buffer_load_dwordx4 v12, s[72:75], s51 offen offset:1024 nt lds\n\t"
      "v_cvt_pk_bf16_f32 v65, v66, v67\n\t"
      "v_exp_f32 v69, v69\n\t"
      "v_mfma_f32_16x16x32_bf16 v[104:107], a[96:99], a[16:19], v[36:39]\n\t"
      "v_add_f32 v70, v70, v32\n\t"
      "v_exp_f32 v70, v70\n\t"
      "v_mfma_f32_16x16x32_bf16 v[104:107], a[100:103], a[20:23], v[104:107]\n\t"
      "v_add_f32 v71, v71, v32\n\t"
      "v_cvt_pk_bf16_f32 v66, v68, v69\n\t"
      "v_mfma_f32_16x16x32_bf16 v[104:107], a[104:107], a[24:27], v[104:107]\n\t"
      "v_exp_f32 v71, v71\n\t"
      "v_add_f32 v72, v72, v36\n\t"
      "v_mfma_f32_16x16x32_bf16 v[104:107], a[108:111], a[28:31], v[104:107]\n\t"
      "buffer_load_dwordx4 v13, s[72:75], s51 offen offset:2048 nt lds\n\t"
      "v_exp_f32 v72, v72\n\t"
      "v_add_f32 v73, v73, v36\n\t"
      "v_cvt_pk_bf16_f32 v67, v70, v71\n\t"
      "v_mfma_f32_16x16x32_bf16 v[108:111], a[112:115], a[16:19], v[36:39]\n\t"
      "v_exp_f32 v73, v73\n\t"
      "v_add_f32 v74, v74, v36\n\t"
      "v_mfma_f32_16x16x32_bf16 v[108:111], a[116:119], a[20:23], v[108:111]\n\t"
      "v_exp_f32 v74, v74\n\t"
      "v_add_f32 v75, v75, v36\n\t"
      "v_mfma_f32_16x16x32_bf16 v[108:111], a[120:123], a[24:27], v[108:111]\n\t"
      "v_cvt_pk_bf16_f32 v68, v72, v73\n\t"
      "v_exp_f32 v75, v75\n\t"
      "v_mfma_f32_16x16x32_bf16 v[108:111], a[124:127], a[28:31], v[108:111]\n\t"
      "buffer_load_dwordx4 v14, s[72:75], s51 offen offset:3072 nt lds\n\t"
      "v_add_f32 v76, v76, v36\n\t"
      "v_exp_f32 v76, v76\n\t"
      "v_mfma_f32_16x16x32_bf16 v[112:115], a[96:99], a[32:35], v[40:43]\n\t"
      "v_add_f32 v77, v77, v36\n\t"
      "v_cvt_pk_bf16_f32 v69, v74, v75\n\t"
      "v_mfma_f32_16x16x32_bf16 v[112:115], a[100:103], a[36:39], v[112:115]\n\t"
      "v_exp_f32 v77, v77\n\t"
      "v_add_f32 v78, v78, v36\n\t"
      "v_mfma_f32_16x16x32_bf16 v[112:115], a[104:107], a[40:43], v[112:115]\n\t"
      "v_exp_f32 v78, v78\n\t"
      "v_add_f32 v79, v79, v36\n\t"
      "v_mfma_f32_16x16x32_bf16 v[112:115], a[108:111], a[44:47], v[112:115]\n\t"
      "v_cvt_pk_bf16_f32 v70, v76, v77\n\t"
      "v_exp_f32 v79, v79\n\t"
      "v_mfma_f32_16x16x32_bf16 v[116:119], a[112:115], a[32:35], v[40:43]\n\t"
      "v_add_f32 v80, v80, v40\n\t"
      "v_exp_f32 v80, v80\n\t"
      "v_mfma_f32_16x16x32_bf16 v[116:119], a[116:119], a[36:39], v[116:119]\n\t"
      "v_add_f32 v81, v81, v40\n\t"
      "v_cvt_pk_bf16_f32 v71, v78, v79\n\t"
      "v_mfma_f32_16x16x32_bf16 v[116:119], a[120:123], a[40:43], v[116:119]\n\t"
      "v_exp_f32 v81, v81\n\t"
      "v_add_f32 v82, v82, v40\n\t"
      "v_mfma_f32_16x16x32_bf16 v[116:119], a[124:127], a[44:47], v[116:119]\n\t"
      "v_exp_f32 v82, v82\n\t"
      "v_add_f32 v83, v83, v40\n\t"
      "v_cvt_pk_bf16_f32 v80, v80, v81\n\t"
      "v_mfma_f32_16x16x32_bf16 v[120:123], a[96:99], a[48:51], v[44:47]\n\t"
      "v_exp_f32 v83, v83\n\t"
      "v_add_f32 v84, v84, v40\n\t"
      "v_mfma_f32_16x16x32_bf16 v[120:123], a[100:103], a[52:55], v[120:123]\n\t"
      "v_exp_f32 v84, v84\n\t"
      "v_add_f32 v85, v85, v40\n\t"
      "v_mfma_f32_16x16x32_bf16 v[120:123], a[104:107], a[56:59], v[120:123]\n\t"
      "v_cvt_pk_bf16_f32 v81, v82, v83\n\t"
      "v_exp_f32 v85, v85\n\t"
      "v_mfma_f32_16x16x32_bf16 v[120:123], a[108:111], a[60:63], v[120:123]\n\t"
      "v_add_f32 v86, v86, v40\n\t"
      "v_exp_f32 v86, v86\n\t"
      "v_mfma_f32_16x16x32_bf16 v[124:127], a[112:115], a[48:51], v[44:47]\n\t"
      "v_add_f32 v87, v87, v40\n\t"
      "v_cvt_pk_bf16_f32 v82, v84, v85\n\t"
      "v_mfma_f32_16x16x32_bf16 v[124:127], a[116:119], a[52:55], v[124:127]\n\t"
      "v_exp_f32 v87, v87\n\t"
      "v_add_f32 v88, v88, v44\n\t"
      "v_mfma_f32_16x16x32_bf16 v[124:127], a[120:123], a[56:59], v[124:127]\n\t"
      "v_exp_f32 v88, v88\n\t"
      "v_add_f32 v89, v89, v44\n\t"
      "v_cvt_pk_bf16_f32 v83, v86, v87\n\t"
      "v_mfma_f32_16x16x32_bf16 v[124:127], a[124:127], a[60:63], v[124:127]\n\t"
      "v_exp_f32 v89, v89\n\t"
      "v_add_f32 v90, v90, v44\n\t"
      "s_add_u32 m0, s49, 49152\n\t"
      "s_nop 0\n\t"
      "buffer_load_dwordx4 v7, s[36:39], s53 offen offset:0 nt lds\n\t"
      "buffer_load_dwordx4 v8, s[36:39], s53 offen offset:1024 nt lds\n\t"
      "buffer_load_dwordx4 v9, s[36:39], s53 offen offset:2048 nt lds\n\t"
      "buffer_load_dwordx4 v10, s[36:39], s53 offen offset:3072 nt lds\n\t"
      "s_add_u32 m0, s44, 49152\n\t"
      "s_nop 0\n\t"
      "buffer_load_dwordx4 v11, s[72:75], s52 offen offset:0 nt lds\n\t"
      "buffer_load_dwordx4 v12, s[72:75], s52 offen offset:1024 nt lds\n\t"
      "buffer_load_dwordx4 v13, s[72:75], s52 offen offset:2048 nt lds\n\t"
      "buffer_load_dwordx4 v14, s[72:75], s52 offen offset:3072 nt lds\n\t"
      "s_waitcnt vmcnt(17)\n\t"
      "s_barrier\n\t"
      "v_add_u32 v4, s59, v4\n\t"
      "v_add_u32 v5, s59, v5\n\t"
      "v_add_u32 v2, s59, v2\n\t"
      "v_add_u32 v3, s59, v3\n\t"
      "ds_read_b64_tr_b16 a[128:129], v4 offset:0\n\t"
      "ds_read_b64_tr_b16 a[130:131], v4 offset:4096\n\t"
      "ds_read_b64_tr_b16 a[132:133], v5 offset:0\n\t"
      "ds_read_b64_tr_b16 a[134:135], v5 offset:4096\n\t"
      "ds_read_b64_tr_b16 a[136:137], v4 offset:512\n\t"
      "ds_read_b64_tr_b16 a[138:139], v4 offset:4608\n\t"
      "ds_read_b64_tr_b16 a[140:141], v5 offset:512\n\t"
      "ds_read_b64_tr_b16 a[142:143], v5 offset:4608\n\t"
      "ds_read_b64_tr_b16 a[144:145], v4 offset:1024\n\t"
      "ds_read_b64_tr_b16 a[146:147], v4 offset:5120\n\t"
      "ds_read_b64_tr_b16 a[148:149], v5 offset:1024\n\t"
      "ds_read_b64_tr_b16 a[150:151], v5 offset:5120\n\t"
      "ds_read_b64_tr_b16 a[152:153], v4 offset:1536\n\t"
      "ds_read_b64_tr_b16 a[154:155], v4 offset:5632\n\t"
      "ds_read_b64_tr_b16 a[156:157], v5 offset:1536\n\t"
      "ds_read_b64_tr_b16 a[158:159], v5 offset:5632\n\t"
      "ds_read_b128 a[64:67], v2 offset:0\n\t"
      "ds_read_b128 a[68:71], v3 offset:0\n\t"
      "ds_read_b128 a[72:75], v2 offset:1024\n\t"
      "ds_read_b128 a[76:79], v3 offset:1024\n\t"
      "ds_read_b128 a[80:83], v2 offset:4096\n\t"
      "ds_read_b128 a[84:87], v3 offset:4096\n\t"
      "ds_read_b128 a[88:91], v2 offset:5120\n\t"
      "ds_read_b128 a[92:95], v3 offset:5120\n\t"
      "v_exp_f32 v90, v90\n\t"
      "v_add_f32 v91, v91, v44\n\t"
      "v_cvt_pk_bf16_f32 v84, v88, v89\n\t"
      "v_exp_f32 v91, v91\n\t"
      "v_add_f32 v92, v92, v44\n\t"
      "v_exp_f32 v92, v92\n\t"
      "v_add_f32 v93, v93, v44\n\t"
      "v_cvt_pk_bf16_f32 v85, v90, v91\n\t"
      "v_exp_f32 v93, v93\n\t"
      "v_add_f32 v94, v94, v44\n\t"
      "v_exp_f32 v94, v94\n\t"
      "v_add_f32 v95, v95, v44\n\t"
      "v_cvt_pk_bf16_f32 v86, v92, v93\n\t"
      "v_exp_f32 v95, v95\n\t"
      "s_nop 0\n\t"
      "v_cvt_pk_bf16_f32 v87, v94, v95\n\t"
      "s_and_b32 s87, %[ob], 3\n\t"
      "s_lshl_b32 s87, s87, 3\n\t"
      "s_lshr_b32 s86, 0x44464442, s87\n\t"
      "s_and_b32 s86, s86, 0xff\n\t"
      "s_mov_b32 s87, 0\n\t"
      "s_mov_b64 vcc, s[86:87]\n\t"
      "s_add_u32 m0, s43, s49\n\t"
      "s_add_u32 s47, s47, 1\n\t"
      "s_cmp_lt_u32 s47, s48\n\t"
      "s_waitcnt lgkmcnt(0)\n\t"
      "s_branch TAIL_%=\n\t"
      "RARE_%=:\n\t"
      "s_and_b32 s88, s86, 15\n\t"
      "s_cbranch_scc0 PX_%=\n\t"
      "PL_%=:\n\t"
      "s_sleep 2\n\t"
      "s_sub_u32 s88, s88, 1\n\t"
      "s_cmp_lg_u32 s88, 0\n\t"
      "s_cbranch_scc1 PL_%=\n\t"
      "PX_%=:\n\t"
      "s_lshr_b64 s[86:87], s[86:87], 4\n\t"
      "s_cmp_eq_u32 s47, s48\n\t"
      "s_mov_b64 vcc, s[86:87]\n\t"
      "s_cbranch_scc0 RB_%=\n\t"
      "v_cmp_ge_i32_e64 s[60:61], 0, v21\n\t"
      "v_cmp_ge_i32_e64 s[62:63], 1, v21\n\t"
      "v_cmp_ge_i32_e64 s[64:65], 2, v21\n\t"
      "v_cmp_ge_i32_e64 s[66:67], 3, v21\n\t"
      "s_nop 2\n\t"
      "v_cndmask_b32_e64 v64, v64, v20, s[60:61]\n\t"
      "v_cndmask_b32_e64 v65, v65, v20, s[62:63]\n\t"
      "v_cndmask_b32_e64 v66, v66, v20, s[64:65]\n\t"
      "v_cndmask_b32_e64 v67, v67, v20, s[66:67]\n\t"
      "v_cndmask_b32_e64 v72, v72, v20, s[60:61]\n\t"
      "v_cndmask_b32_e64 v73, v73, v20, s[62:63]\n\t"
      "v_cndmask_b32_e64 v74, v74, v20, s[64:65]\n\t"
      "v_cndmask_b32_e64 v75, v75, v20, s[66:67]\n\t"
      "v_cndmask_b32_e64 v80, v80, v20, s[60:61]\n\t"
      "v_cndmask_b32_e64 v81, v81, v20, s[62:63]\n\t"
      "v_cndmask_b32_e64 v82, v82, v20, s[64:65]\n\t"
      "v_cndmask_b32_e64 v83, v83, v20, s[66:67]\n\t"
      "v_cndmask_b32_e64 v88, v88, v20, s[60:61]\n\t"
      "v_cndmask_b32_e64 v89, v89, v20, s[62:63]\n\t"
      "v_cndmask_b32_e64 v90, v90, v20, s[64:65]\n\t"
      "v_cndmask_b32_e64 v91, v91, v20, s[66:67]\n\t"
      "v_cmp_ge_i32_e64 s[60:61], 16, v21\n\t"
      "v_cmp_ge_i32_e64 s[62:63], 17, v21\n\t"
      "v_cmp_ge_i32_e64 s[64:65], 18, v21\n\t"
      "v_cmp_ge_i32_e64 s[66:67], 19, v21\n\t"
      "v_cndmask_b32_e64 v68, v68, v20, s[60:61]\n\t"
      "v_cndmask_b32_e64 v69, v69, v20, s[62:63]\n\t"
      "v_cndmask_b32_e64 v70, v70, v20, s[64:65]\n\t"
      "v_cndmask_b32_e64 v71, v71, v20, s[66:67]\n\t"
      "v_cndmask_b32_e64 v76, v76, v20, s[60:61]\n\t"
      "v_cndmask_b32_e64 v77, v77, v20, s[62:63]\n\t"
      "v_cndmask_b32_e64 v78, v78, v20, s[64:65]\n\t"
      "v_cndmask_b32_e64 v79, v79, v20, s[66:67]\n\t"
      "v_cndmask_b32_e64 v84, v84, v20, s[60:61]\n\t"
      "v_cndmask_b32_e64 v85, v85, v20, s[62:63]\n\t"
      "v_cndmask_b32_e64 v86, v86, v20, s[64:65]\n\t"
      "v_cndmask_b32_e64 v87, v87, v20, s[66:67]\n\t"
      "v_cndmask_b32_e64 v92, v92, v20, s[60:61]\n\t"
      "v_cndmask_b32_e64 v93, v93, v20, s[62:63]\n\t"
      "v_cndmask_b32_e64 v94, v94, v20, s[64:65]\n\t"
      "v_cndmask_b32_e64 v95, v95, v20, s[66:67]\n\t"
      "s_branch RB_%=\n\t"
      ".p2align 6\n\t"
      "LOOP_%=:\n\t"
      "v_mfma_f32_16x16x32_bf16 v[128:131], a[128:131], v[64:67], v[128:131]\n\t"
      "ds_read_b64_tr_b16 a[160:161], v4 offset:8192\n\t"
      "ds_read_b64_tr_b16 a[162:163], v4 offset:12288\n\t"
      "v_mfma_f32_16x16x32_bf16 v[132:135], a[132:135], v[64:67], v[132:135]\n\t"
      "v_exp_f32 v96, v96\n\t"
      "v_mfma_f32_16x16x32_bf16 v[136:139], a[136:139], v[64:67], v[136:139]\n\t"
      "buffer_load_dwordx4 v7, s[36:39], s45 offen offset:0 nt lds\n\t"
      "v_mfma_f32_16x16x32_bf16 v[140:143], a[140:143], v[64:67], v[140:143]\n\t"
      "v_exp_f32 v97, v97\n\t"
      "v_mfma_f32_16x16x32_bf16 v[144:147], a[144:147], v[64:67], v[144:147]\n\t"
      "ds_read_b64_tr_b16 a[164:165], v5 offset:8192\n\t"
      "v_cvt_pk_bf16_f32 v96, v96, v97\n\t"
      "v_mfma_f32_16x16x32_bf16 v[148:151], a[148:151], v[64:67], v[148:151]\n\t"
      "v_exp_f32 v98, v98\n\t"
      "v_mfma_f32_16x16x32_bf16 v[152:155], a[152:155], v[64:67], v[152:155]\n\t"
      "ds_read_b64_tr_b16 a[166:167], v5 offset:12288\n\t"
      "ds_read_b64_tr_b16 a[168:169], v4 offset:8704\n\t"
      "v_mfma_f32_16x16x32_bf16 v[156:159], a[156:159], v[64:67], v[156:159]\n\t"
      "v_exp_f32 v99, v99\n\t"
      "v_mfma_f32_16x16x32_bf16 v[160:163], a[128:131], v[68:71], v[160:163]\n\t"
      "ds_read_b64_tr_b16 a[170:171], v4 offset:12800\n\t"
      "v_cvt_pk_bf16_f32 v97, v98, v99\n\t"
      "v_mfma_f32_16x16x32_bf16 v[164:167], a[132:135], v[68:71], v[164:167]\n\t"
      "v_exp_f32 v100, v100\n\t"
      "v_mfma_f32_16x16x32_bf16 v[168:171], a[136:139], v[68:71], v[168:171]\n\t"
      "buffer_load_dwordx4 v8, s[36:39], s45 offen offset:1024 nt lds\n\t"
      "v_mfma_f32_16x16x32_bf16 v[172:175], a[140:143], v[68:71], v[172:175]\n\t"
      "v_exp_f32 v101, v101\n\t"
      "v_mfma_f32_16x16x32_bf16 v[176:179], a[144:147], v[68:71], v[176:179]\n\t"
      "ds_read_b64_tr_b16 a[172:173], v5 offset:8704\n\t"
      "v_cvt_pk_bf16_f32 v98, v100, v101\n\t"
      "v_mfma_f32_16x16x32_bf16 v[180:183], a[148:151], v[68:71], v[180:183]\n\t"
      "v_exp_f32 v102, v102\n\t"
      "v_mfma_f32_16x16x32_bf16 v[184:187], a[152:155], v[68:71], v[184:187]\n\t"
      "ds_read_b64_tr_b16 a[174:175], v5 offset:12800\n\t"
      "ds_read_b64_tr_b16 a[176:177], v4 offset:9216\n\t"
      "v_mfma_f32_16x16x32_bf16 v[188:191], a[156:159], v[68:71], v[188:191]\n\t"
      "v_exp_f32 v103, v103\n\t"
      "v_smfmac_f32_16x16x64_bf16 v[48:51], a[192:195], v[64:71], v6\n\t"
      "ds_read_b64_tr_b16 a[178:179], v4 offset:13312\n\t"
      "v_cvt_pk_bf16_f32 v99, v102, v103\n\t"
      "v_mfma_f32_16x16x32_bf16 v[192:195], a[128:131], v[80:83], v[192:195]\n\t"
      "v_exp_f32 v104, v104\n\t"
      "v_mfma_f32_16x16x32_bf16 v[196:199], a[132:135], v[80:83], v[196:199]\n\t"
      "buffer_load_dwordx4 v9, s[36:39], s45 offen offset:2048 nt lds\n\t"
      "v_mfma_f32_16x16x32_bf16 v[200:203], a[136:139], v[80:83], v[200:203]\n\t"
      "v_exp_f32 v105, v105\n\t"
      "v_mfma_f32_16x16x32_bf16 v[204:207], a[140:143], v[80:83], v[204:207]\n\t"
      "ds_read_b64_tr_b16 a[180:181], v5 offset:9216\n\t"
      "v_cvt_pk_bf16_f32 v100, v104, v105\n\t"
      "v_mfma_f32_16x16x32_bf16 v[208:211], a[144:147], v[80:83], v[208:211]\n\t"
      "v_exp_f32 v106, v106\n\t"
      "v_mfma_f32_16x16x32_bf16 v[212:215], a[148:151], v[80:83], v[212:215]\n\t"
      "ds_read_b64_tr_b16 a[182:183], v5 offset:13312\n\t"
      "ds_read_b64_tr_b16 a[184:185], v4 offset:9728\n\t"
      "v_mfma_f32_16x16x32_bf16 v[216:219], a[152:155], v[80:83], v[216:219]\n\t"
      "v_exp_f32 v107, v107\n\t"
      "v_mfma_f32_16x16x32_bf16 v[220:223], a[156:159], v[80:83], v[220:223]\n\t"
      "ds_read_b64_tr_b16 a[186:187], v4 offset:13824\n\t"
      "v_cvt_pk_bf16_f32 v101, v106, v107\n\t"
      "v_mfma_f32_16x16x32_bf16 v[224:227], a[128:131], v[84:87], v[224:227]\n\t"
      "v_exp_f32 v108, v108\n\t"
      "v_mfma_f32_16x16x32_bf16 v[228:231], a[132:135], v[84:87], v[228:231]\n\t"
      "buffer_load_dwordx4 v10, s[36:39], s45 offen offset:3072 nt lds\n\t"
      "v_mfma_f32_16x16x32_bf16 v[232:235], a[136:139], v[84:87], v[232:235]\n\t"
      "v_exp_f32 v109, v109\n\t"
      "v_mfma_f32_16x16x32_bf16 v[236:239], a[140:143], v[84:87], v[236:239]\n\t"
      "s_add_u32 m0, s43, s44\n\t"
      "v_cvt_pk_bf16_f32 v102, v108, v109\n\t"
      "v_mfma_f32_16x16x32_bf16 v[240:243], a[144:147], v[84:87], v[240:243]\n\t"
      "v_exp_f32 v110, v110\n\t"
      "v_mfma_f32_16x16x32_bf16 v[244:247], a[148:151], v[84:87], v[244:247]\n\t"
      "ds_read_b64_tr_b16 a[188:189], v5 offset:9728\n\t"
      "ds_read_b64_tr_b16 a[190:191], v5 offset:13824\n\t"
      "v_mfma_f32_16x16x32_bf16 v[248:251], a[152:155], v[84:87], v[248:251]\n\t"
      "v_exp_f32 v111, v111\n\t"
      "v_mfma_f32_16x16x32_bf16 v[252:255], a[156:159], v[84:87], v[252:255]\n\t"
      "ds_read_b128 a[96:99], v2 offset:8192\n\t"
      "v_cvt_pk_bf16_f32 v103, v110, v111\n\t"
      "v_smfmac_f32_16x16x64_bf16 v[48:51], a[196:199], v[80:87], v6\n\t"
      "v_exp_f32 v112, v112\n\t"
      "v_mfma_f32_16x16x32_bf16 v[64:67], a[64:67], a[0:3], v[32:35]\n\t"
      "buffer_load_dwordx4 v11, s[72:75], s46 offen offset:0 nt lds\n\t"
      "v_mfma_f32_16x16x32_bf16 v[64:67], a[68:71], a[4:7], v[64:67]\n\t"
      "v_exp_f32 v113, v113\n\t"
      "v_mfma_f32_16x16x32_bf16 v[64:67], a[72:75], a[8:11], v[64:67]\n\t"
      "ds_read_b128 a[100:103], v3 offset:8192\n\t"
      "v_cvt_pk_bf16_f32 v112, v112, v113\n\t"
      "v_mfma_f32_16x16x32_bf16 v[64:67], a[76:79], a[12:15], v[64:67]\n\t"
      "v_exp_f32 v114, v114\n\t"
      "v_mfma_f32_16x16x32_bf16 v[68:71], a[80:83], a[0:3], v[32:35]\n\t"
      "ds_read_b128 a[104:107], v2 offset:9216\n\t"
      "ds_read_b128 a[108:111], v3 offset:9216\n\t"
      "v_mfma_f32_16x16x32_bf16 v[68:71], a[84:87], a[4:7], v[68:71]\n\t"
      "v_exp_f32 v115, v115\n\t"
      "v_mfma_f32_16x16x32_bf16 v[68:71], a[88:91], a[8:11], v[68:71]\n\t"
      "ds_read_b128 a[112:115], v2 offset:12288\n\t"
      "v_cvt_pk_bf16_f32 v113, v114, v115\n\t"
      "v_mfma_f32_16x16x32_bf16 v[68:71], a[92:95], a[12:15], v[68:71]\n\t"
      "v_exp_f32 v116, v116\n\t"
      "v_mfma_f32_16x16x32_bf16 v[72:75], a[64:67], a[16:19], v[36:39]\n\t"
      "buffer_load_dwordx4 v12, s[72:75], s46 offen offset:1024 nt lds\n\t"
      "v_mfma_f32_16x16x32_bf16 v[72:75], a[68:71], a[20:23], v[72:75]\n\t"
      "v_exp_f32 v117, v117\n\t"
      "v_mfma_f32_16x16x32_bf16 v[72:75], a[72:75], a[24:27], v[72:75]\n\t"
      "ds_read_b128 a[116:119], v3 offset:12288\n\t"
      "v_cvt_pk_bf16_f32 v114, v116, v117\n\t"
      "v_mfma_f32_16x16x32_bf16 v[72:75], a[76:79], a[28:31], v[72:75]\n\t"
      "v_exp_f32 v118, v118\n\t"
      "v_mfma_f32_16x16x32_bf16 v[76:79], a[80:83], a[16:19], v[36:39]\n\t"
      "ds_read_b128 a[120:123], v2 offset:13312\n\t"
      "ds_read_b128 a[124:127], v3 offset:13312\n\t"
      "v_mfma_f32_16x16x32_bf16 v[76:79], a[84:87], a[20:23], v[76:79]\n\t"
      "v_exp_f32 v119, v119\n\t"
      "v_mfma_f32_16x16x32_bf16 v[76:79], a[88:91], a[24:27], v[76:79]\n\t"
      "v_cvt_pk_bf16_f32 v115, v118, v119\n\t"
      "v_add_u32 v4, s59, v4\n\t"
      "v_mfma_f32_16x16x32_bf16 v[76:79], a[92:95], a[28:31], v[76:79]\n\t"
      "v_exp_f32 v120, v120\n\t"
      "v_mfma_f32_16x16x32_bf16 v[80:83], a[64:67], a[32:35], v[40:43]\n\t"
      "buffer_load_dwordx4 v13, s[72:75], s46 offen offset:2048 nt lds\n\t"
      "v_mfma_f32_16x16x32_bf16 v[80:83], a[68:71], a[36:39], v[80:83]\n\t"
      "v_exp_f32 v121, v121\n\t"
      "v_mfma_f32_16x16x32_bf16 v[80:83], a[72:75], a[40:43], v[80:83]\n\t"
      "v_cvt_pk_bf16_f32 v116, v120, v121\n\t"
      "v_add_u32 v5, s59, v5\n\t"
      "v_mfma_f32_16x16x32_bf16 v[80:83], a[76:79], a[44:47], v[80:83]\n\t"
      "v_exp_f32 v122, v122\n\t"
      "v_mfma_f32_16x16x32_bf16 v[84:87], a[80:83], a[32:35], v[40:43]\n\t"
      "v_add_u32 v2, s59, v2\n\t"
      "v_add_u32 v3, s59, v3\n\t"
      "v_mfma_f32_16x16x32_bf16 v[84:87], a[84:87], a[36:39], v[84:87]\n\t"
      "v_exp_f32 v123, v123\n\t"
      "v_mfma_f32_16x16x32_bf16 v[84:87], a[88:91], a[40:43], v[84:87]\n\t"
      "v_cvt_pk_bf16_f32 v117, v122, v123\n\t"
      "v_mfma_f32_16x16x32_bf16 v[84:87], a[92:95], a[44:47], v[84:87]\n\t"
      "v_exp_f32 v124, v124\n\t"
      "v_mfma_f32_16x16x32_bf16 v[88:91], a[64:67], a[48:51], v[44:47]\n\t"
      "buffer_load_dwordx4 v14, s[72:75], s46 offen offset:3072 nt lds\n\t"
      "v_mfma_f32_16x16x32_bf16 v[88:91], a[68:71], a[52:55], v[88:91]\n\t"
      "v_exp_f32 v125, v125\n\t"
      "v_mfma_f32_16x16x32_bf16 v[88:91], a[72:75], a[56:59], v[88:91]\n\t"
      "v_cvt_pk_bf16_f32 v118, v124, v125\n\t"
      "v_mfma_f32_16x16x32_bf16 v[88:91], a[76:79], a[60:63], v[88:91]\n\t"
      "v_exp_f32 v126, v126\n\t"
      "v_mfma_f32_16x16x32_bf16 v[92:95], a[80:83], a[48:51], v[44:47]\n\t"
      "v_mfma_f32_16x16x32_bf16 v[92:95], a[84:87], a[52:55], v[92:95]\n\t"
      "v_exp_f32 v127, v127\n\t"
      "v_mfma_f32_16x16x32_bf16 v[92:95], a[88:91], a[56:59], v[92:95]\n\t"
      "v_cvt_pk_bf16_f32 v119, v126, v127\n\t"
      "s_waitcnt lgkmcnt(0)\n\t"
      "v_mfma_f32_16x16x32_bf16 v[92:95], a[92:95], a[60:63], v[92:95]\n\t"
      "s_cbranch_vccnz RARE_%=\n\t"
      "RB_%=:\n\t"
      "v_mfma_f32_16x16x32_bf16 v[128:131], a[160:163], v[96:99], v[128:131]\n\t"
      "s_waitcnt vmcnt(16)\n\t"
      "s_barrier\n\t"
      "v_mfma_f32_16x16x32_bf16 v[132:135], a[164:167], v[96:99], v[132:135]\n\t"
      "v_exp_f32 v64, v64\n\t"
      "v_mfma_f32_16x16x32_bf16 v[136:139], a[168:171], v[96:99], v[136:139]\n\t"
      "ds_read_b64_tr_b16 a[128:129], v4 offset:0\n\t"
      "ds_read_b64_tr_b16 a[130:131], v4 offset:4096\n\t"
      "v_mfma_f32_16x16x32_bf16 v[140:143], a[172:175], v[96:99], v[140:143]\n\t"
      "v_exp_f32 v65, v65\n\t"
      "v_mfma_f32_16x16x32_bf16 v[144:147], a[176:179], v[96:99], v[144:147]\n\t"
      "s_mov_b32 s46, s45\n\t"
      "v_cvt_pk_bf16_f32 v64, v64, v65\n\t"
      "v_mfma_f32_16x16x32_bf16 v[148:151], a[180:183], v[96:99], v[148:151]\n\t"
      "v_exp_f32 v66, v66\n\t"
      "v_mfma_f32_16x16x32_bf16 v[152:155], a[184:187], v[96:99], v[152:155]\n\t"
      "ds_read_b64_tr_b16 a[132:133], v5 offset:0\n\t"
      "ds_read_b64_tr_b16 a[134:135], v5 offset:4096\n\t"
      "v_mfma_f32_16x16x32_bf16 v[156:159], a[188:191], v[96:99], v[156:159]\n\t"
      "v_exp_f32 v67, v67\n\t"
      "v_mfma_f32_16x16x32_bf16 v[160:163], a[160:163], v[100:103], v[160:163]\n\t"
      "s_add_u32 s43, s43, 0x4000\n\t"
      "v_cvt_pk_bf16_f32 v65, v66, v67\n\t"
      "v_mfma_f32_16x16x32_bf16 v[164:167], a[164:167], v[100:103], v[164:167]\n\t"
      "v_exp_f32 v68, v68\n\t"
      "v_mfma_f32_16x16x32_bf16 v[168:171], a[168:171], v[100:103], v[168:171]\n\t"
      "ds_read_b64_tr_b16 a[136:137], v4 offset:512\n\t"
      "ds_read_b64_tr_b16 a[138:139], v4 offset:4608\n\t"
      "v_mfma_f32_16x16x32_bf16 v[172:175], a[172:175], v[100:103], v[172:175]\n\t"
      "v_exp_f32 v69, v69\n\t"
      "v_mfma_f32_16x16x32_bf16 v[176:179], a[176:179], v[100:103], v[176:179]\n\t"
      "v_cvt_pk_bf16_f32 v66, v68, v69\n\t"
      "v_mfma_f32_16x16x32_bf16 v[180:183], a[180:183], v[100:103], v[180:183]\n\t"
      "v_exp_f32 v70, v70\n\t"
      "v_mfma_f32_16x16x32_bf16 v[184:187], a[184:187], v[100:103], v[184:187]\n\t"
      "s_cmp_eq_u32 s43, 81920\n\t"
      "ds_read_b64_tr_b16 a[140:141], v5 offset:512\n\t"
      "v_mfma_f32_16x16x32_bf16 v[188:191], a[188:191], v[100:103], v[188:191]\n\t"
      "v_exp_f32 v71, v71\n\t"
      "v_smfmac_f32_16x16x64_bf16 v[48:51], a[192:195], v[96:103], v6\n\t"
      "ds_read_b64_tr_b16 a[142:143], v5 offset:4608\n\t"
      "v_cvt_pk_bf16_f32 v67, v70, v71\n\t"
      "v_mfma_f32_16x16x32_bf16 v[192:195], a[160:163], v[112:115], v[192:195]\n\t"
      "v_exp_f32 v72, v72\n\t"
      "v_mfma_f32_16x16x32_bf16 v[196:199], a[164:167], v[112:115], v[196:199]\n\t"
      "ds_read_b64_tr_b16 a[144:145], v4 offset:1024\n\t"
      "v_mfma_f32_16x16x32_bf16 v[200:203], a[168:171], v[112:115], v[200:203]\n\t"
      "v_exp_f32 v73, v73\n\t"
      "v_mfma_f32_16x16x32_bf16 v[204:207], a[172:175], v[112:115], v[204:207]\n\t"
      "ds_read_b64_tr_b16 a[146:147], v4 offset:5120\n\t"
      "v_cvt_pk_bf16_f32 v68, v72, v73\n\t"
      "v_mfma_f32_16x16x32_bf16 v[208:211], a[176:179], v[112:115], v[208:211]\n\t"
      "v_exp_f32 v74, v74\n\t"
      "v_mfma_f32_16x16x32_bf16 v[212:215], a[180:183], v[112:115], v[212:215]\n\t"
      "s_cselect_b32 s43, 0, s43\n\t"
      "ds_read_b64_tr_b16 a[148:149], v5 offset:1024\n\t"
      "v_mfma_f32_16x16x32_bf16 v[216:219], a[184:187], v[112:115], v[216:219]\n\t"
      "v_exp_f32 v75, v75\n\t"
      "v_mfma_f32_16x16x32_bf16 v[220:223], a[188:191], v[112:115], v[220:223]\n\t"
      "ds_read_b64_tr_b16 a[150:151], v5 offset:5120\n\t"
      "v_cvt_pk_bf16_f32 v69, v74, v75\n\t"
      "v_mfma_f32_16x16x32_bf16 v[224:227], a[160:163], v[116:119], v[224:227]\n\t"
      "v_exp_f32 v76, v76\n\t"
      "v_mfma_f32_16x16x32_bf16 v[228:231], a[164:167], v[116:119], v[228:231]\n\t"
      "s_mov_b32 s42, s41\n\t"
      "ds_read_b64_tr_b16 a[152:153], v4 offset:1536\n\t"
      "v_mfma_f32_16x16x32_bf16 v[232:235], a[168:171], v[116:119], v[232:235]\n\t"
      "v_exp_f32 v77, v77\n\t"
      "v_mfma_f32_16x16x32_bf16 v[236:239], a[172:175], v[116:119], v[236:239]\n\t"
      "ds_read_b64_tr_b16 a[154:155], v4 offset:5632\n\t"
      "v_cvt_pk_bf16_f32 v70, v76, v77\n\t"
      "v_mfma_f32_16x16x32_bf16 v[240:243], a[176:179], v[116:119], v[240:243]\n\t"
      "v_exp_f32 v78, v78\n\t"
      "v_mfma_f32_16x16x32_bf16 v[244:247], a[180:183], v[116:119], v[244:247]\n\t"
      "ds_read_b64_tr_b16 a[156:157], v5 offset:1536\n\t"
      "v_mfma_f32_16x16x32_bf16 v[248:251], a[184:187], v[116:119], v[248:251]\n\t"
      "v_exp_f32 v79, v79\n\t"
      "v_mfma_f32_16x16x32_bf16 v[252:255], a[188:191], v[116:119], v[252:255]\n\t"
      "ds_read_b64_tr_b16 a[158:159], v5 offset:5632\n\t"
      "v_cvt_pk_bf16_f32 v71, v78, v79\n\t"
      "v_smfmac_f32_16x16x64_bf16 v[48:51], a[196:199], v[112:119], v6\n\t"
      "v_exp_f32 v80, v80\n\t"
      "v_mfma_f32_16x16x32_bf16 v[96:99], a[96:99], a[0:3], v[32:35]\n\t"
      "s_add_u32 s41, s41, 0x4000\n\t"
      "ds_read_b128 a[64:67], v2 offset:0\n\t"
      "v_mfma_f32_16x16x32_bf16 v[96:99], a[100:103], a[4:7], v[96:99]\n\t"
      "v_exp_f32 v81, v81\n\t"
      "v_mfma_f32_16x16x32_bf16 v[96:99], a[104:107], a[8:11], v[96:99]\n\t"
      "ds_read_b128 a[68:71], v3 offset:0\n\t"
      "v_cvt_pk_bf16_f32 v80, v80, v81\n\t"
      "v_mfma_f32_16x16x32_bf16 v[96:99], a[108:111], a[12:15], v[96:99]\n\t"
      "v_exp_f32 v82, v82\n\t"
      "v_mfma_f32_16x16x32_bf16 v[100:103], a[112:115], a[0:3], v[32:35]\n\t"
      "ds_read_b128 a[72:75], v2 offset:1024\n\t"
      "v_mfma_f32_16x16x32_bf16 v[100:103], a[116:119], a[4:7], v[100:103]\n\t"
      "v_exp_f32 v83, v83\n\t"
      "v_mfma_f32_16x16x32_bf16 v[100:103], a[120:123], a[8:11], v[100:103]\n\t"
      "s_cmp_eq_u32 s41, 81920\n\t"
      "v_cvt_pk_bf16_f32 v81, v82, v83\n\t"
      "v_mfma_f32_16x16x32_bf16 v[100:103], a[124:127], a[12:15], v[100:103]\n\t"
      "v_exp_f32 v84, v84\n\t"
      "v_mfma_f32_16x16x32_bf16 v[104:107], a[96:99], a[16:19], v[36:39]\n\t"
      "ds_read_b128 a[76:79], v3 offset:1024\n\t"
      "ds_read_b128 a[80:83], v2 offset:4096\n\t"
      "v_mfma_f32_16x16x32_bf16 v[104:107], a[100:103], a[20:23], v[104:107]\n\t"
      "v_exp_f32 v85, v85\n\t"
      "v_mfma_f32_16x16x32_bf16 v[104:107], a[104:107], a[24:27], v[104:107]\n\t"
      "s_cselect_b32 s41, 0, s41\n\t"
      "v_cvt_pk_bf16_f32 v82, v84, v85\n\t"
      "v_mfma_f32_16x16x32_bf16 v[104:107], a[108:111], a[28:31], v[104:107]\n\t"
      "v_exp_f32 v86, v86\n\t"
      "v_mfma_f32_16x16x32_bf16 v[108:111], a[112:115], a[16:19], v[36:39]\n\t"
      "ds_read_b128 a[84:87], v3 offset:4096\n\t"
      "ds_read_b128 a[88:91], v2 offset:5120\n\t"
      "v_mfma_f32_16x16x32_bf16 v[108:111], a[116:119], a[20:23], v[108:111]\n\t"
      "v_exp_f32 v87, v87\n\t"
      "v_mfma_f32_16x16x32_bf16 v[108:111], a[120:123], a[24:27], v[108:111]\n\t"
      "v_cvt_pk_bf16_f32 v83, v86, v87\n\t"
      "v_mfma_f32_16x16x32_bf16 v[108:111], a[124:127], a[28:31], v[108:111]\n\t"
      "v_exp_f32 v88, v88\n\t"
      "v_mfma_f32_16x16x32_bf16 v[112:115], a[96:99], a[32:35], v[40:43]\n\t"
      "s_sub_u32 s59, s41, s42\n\t"
      "ds_read_b128 a[92:95], v3 offset:5120\n\t"
      "v_mfma_f32_16x16x32_bf16 v[112:115], a[100:103], a[36:39], v[112:115]\n\t"
      "v_exp_f32 v89, v89\n\t"
      "v_mfma_f32_16x16x32_bf16 v[112:115], a[104:107], a[40:43], v[112:115]\n\t"
      "v_cvt_pk_bf16_f32 v84, v88, v89\n\t"
      "v_mfma_f32_16x16x32_bf16 v[112:115], a[108:111], a[44:47], v[112:115]\n\t"
      "v_exp_f32 v90, v90\n\t"
      "v_mfma_f32_16x16x32_bf16 v[116:119], a[112:115], a[32:35], v[40:43]\n\t"
      "s_add_u32 m0, s43, s49\n\t"
      "v_mfma_f32_16x16x32_bf16 v[116:119], a[116:119], a[36:39], v[116:119]\n\t"
      "v_exp_f32 v91, v91\n\t"
      "v_mfma_f32_16x16x32_bf16 v[116:119], a[120:123], a[40:43], v[116:119]\n\t"
      "v_cvt_pk_bf16_f32 v85, v90, v91\n\t"
      "v_mfma_f32_16x16x32_bf16 v[116:119], a[124:127], a[44:47], v[116:119]\n\t"
      "v_exp_f32 v92, v92\n\t"
      "v_mfma_f32_16x16x32_bf16 v[120:123], a[96:99], a[48:51], v[44:47]\n\t"
      "s_add_u32 s47, s47, 1\n\t"
      "v_mfma_f32_16x16x32_bf16 v[120:123], a[100:103], a[52:55], v[120:123]\n\t"
      "v_exp_f32 v93, v93\n\t"
      "v_mfma_f32_16x16x32_bf16 v[120:123], a[104:107], a[56:59], v[120:123]\n\t"
      "v_cvt_pk_bf16_f32 v86, v92, v93\n\t"
      "v_readlane_b32 s45, v15, s47\n\t"
      "v_mfma_f32_16x16x32_bf16 v[120:123], a[108:111], a[60:63], v[120:123]\n\t"
      "v_exp_f32 v94, v94\n\t"
      "v_mfma_f32_16x16x32_bf16 v[124:127], a[112:115], a[48:51], v[44:47]\n\t"
      "s_cmp_lt_u32 s47, s48\n\t"
      "v_mfma_f32_16x16x32_bf16 v[124:127], a[116:119], a[52:55], v[124:127]\n\t"
      "v_exp_f32 v95, v95\n\t"
      "v_mfma_f32_16x16x32_bf16 v[124:127], a[120:123], a[56:59], v[124:127]\n\t"
      "v_cvt_pk_bf16_f32 v87, v94, v95\n\t"
      "s_waitcnt lgkmcnt(0)\n\t"
      "v_mfma_f32_16x16x32_bf16 v[124:127], a[124:127], a[60:63], v[124:127]\n\t"
      "TAIL_%=:\n\t"
      "s_cbranch_scc1 LOOP_%=\n\t"
      "TAIL2_%=:\n\t"
      "s_cmp_eq_u32 s47, s48\n\t"
      "s_mov_b64 vcc, s[68:69]\n\t"
      "s_cbranch_scc1 LOOP_%=\n\t"
      "v_mfma_f32_16x16x32_bf16 v[128:131], a[128:131], v[64:67], v[128:131]\n\t"
      "ds_read_b64_tr_b16 a[160:161], v4 offset:8192\n\t"
      "ds_read_b64_tr_b16 a[162:163], v4 offset:12288\n\t"
      "v_mfma_f32_16x16x32_bf16 v[132:135], a[132:135], v[64:67], v[132:135]\n\t"
      "s_cbranch_vccnz MKD1_%=\n\t"
      "MBD1_%=:\n\t"
      "ds_read_b64_tr_b16 a[164:165], v5 offset:8192\n\t"
      "v_exp_f32 v96, v96\n\t"
      "v_exp_f32 v97, v97\n\t"
      "v_exp_f32 v98, v98\n\t"
      "v_mfma_f32_16x16x32_bf16 v[136:139], a[136:139], v[64:67], v[136:139]\n\t"
      "ds_read_b64_tr_b16 a[166:167], v5 offset:12288\n\t"
      "ds_read_b64_tr_b16 a[168:169], v4 offset:8704\n\t"
      "v_mfma_f32_16x16x32_bf16 v[140:143], a[140:143], v[64:67], v[140:143]\n\t"
      "ds_read_b64_tr_b16 a[170:171], v4 offset:12800\n\t"
      "v_cvt_pk_bf16_f32 v96, v96, v97\n\t"
      "v_mfma_f32_16x16x32_bf16 v[144:147], a[144:147], v[64:67], v[144:147]\n\t"
      "ds_read_b64_tr_b16 a[172:173], v5 offset:8704\n\t"
      "ds_read_b64_tr_b16 a[174:175], v5 offset:12800\n\t"
      "v_mfma_f32_16x16x32_bf16 v[148:151], a[148:151], v[64:67], v[148:151]\n\t"
      "ds_read_b64_tr_b16 a[176:177], v4 offset:9216\n\t"
      "v_mfma_f32_16x16x32_bf16 v[152:155], a[152:155], v[64:67], v[152:155]\n\t"
      "ds_read_b64_tr_b16 a[178:179], v4 offset:13312\n\t"
      "ds_read_b64_tr_b16 a[180:181], v5 offset:9216\n\t"
      "v_mfma_f32_16x16x32_bf16 v[156:159], a[156:159], v[64:67], v[156:159]\n\t"
      "v_exp_f32 v99, v99\n\t"
      "v_mfma_f32_16x16x32_bf16 v[160:163], a[128:131], v[68:71], v[160:163]\n\t"
      "ds_read_b64_tr_b16 a[182:183], v5 offset:13312\n\t"
      "ds_read_b64_tr_b16 a[184:185], v4 offset:9728\n\t"
      "v_mfma_f32_16x16x32_bf16 v[164:167], a[132:135], v[68:71], v[164:167]\n\t"
      "v_exp_f32 v100, v100\n\t"
      "v_mfma_f32_16x16x32_bf16 v[168:171], a[136:139], v[68:71], v[168:171]\n\t"
      "ds_read_b64_tr_b16 a[186:187], v4 offset:13824\n\t"
      "ds_read_b64_tr_b16 a[188:189], v5 offset:9728\n\t"
      "v_mfma_f32_16x16x32_bf16 v[172:175], a[140:143], v[68:71], v[172:175]\n\t"
      "v_cvt_pk_bf16_f32 v97, v98, v99\n\t"
      "v_exp_f32 v101, v101\n\t"
      "v_mfma_f32_16x16x32_bf16 v[176:179], a[144:147], v[68:71], v[176:179]\n\t"
      "ds_read_b64_tr_b16 a[190:191], v5 offset:13824\n\t"
      "v_mfma_f32_16x16x32_bf16 v[180:183], a[148:151], v[68:71], v[180:183]\n\t"
      "v_exp_f32 v102, v102\n\t"
      "v_mfma_f32_16x16x32_bf16 v[184:187], a[152:155], v[68:71], v[184:187]\n\t"
      "v_cvt_pk_bf16_f32 v98, v100, v101\n\t"
      "v_exp_f32 v103, v103\n\t"
      "v_mfma_f32_16x16x32_bf16 v[188:191], a[156:159], v[68:71], v[188:191]\n\t"
      "v_exp_f32 v104, v104\n\t"
      "v_cvt_pk_bf16_f32 v99, v102, v103\n\t"
      "v_smfmac_f32_16x16x64_bf16 v[48:51], a[192:195], v[64:71], v6\n\t"
      "s_waitcnt lgkmcnt(0)\n\t"
      "v_mfma_f32_16x16x32_bf16 v[128:131], a[160:163], v[96:99], v[128:131]\n\t"
      "v_exp_f32 v105, v105\n\t"
      "v_mfma_f32_16x16x32_bf16 v[132:135], a[164:167], v[96:99], v[132:135]\n\t"
      "v_exp_f32 v106, v106\n\t"
      "v_mfma_f32_16x16x32_bf16 v[136:139], a[168:171], v[96:99], v[136:139]\n\t"
      "v_cvt_pk_bf16_f32 v100, v104, v105\n\t"
      "v_mfma_f32_16x16x32_bf16 v[140:143], a[172:175], v[96:99], v[140:143]\n\t"
      "s_bitcmp1_b32 s80, 0\n\t"
      "s_cbranch_scc1 SK0_%=\n\t"
      "v_cvt_pk_bf16_f32 v128, v128, v129\n\t"
      "v_cvt_pk_bf16_f32 v129, v130, v131\n\t"
      "v_cvt_pk_bf16_f32 v130, v132, v133\n\t"
      "v_cvt_pk_bf16_f32 v131, v134, v135\n\t"
      "buffer_store_dwordx4 v[128:131], v1, s[76:79], 0 offen offset:0 sc1\n\t"
      "SK0_%=:\n\t"
      "v_mfma_f32_16x16x32_bf16 v[144:147], a[176:179], v[96:99], v[144:147]\n\t"
      "v_exp_f32 v107, v107\n\t"
      "v_mfma_f32_16x16x32_bf16 v[148:151], a[180:183], v[96:99], v[148:151]\n\t"
      "s_bitcmp1_b32 s80, 1\n\t"
      "s_cbranch_scc1 SK1_%=\n\t"
      "v_cvt_pk_bf16_f32 v136, v136, v137\n\t"
      "v_cvt_pk_bf16_f32 v137, v138, v139\n\t"
      "v_cvt_pk_bf16_f32 v138, v140, v141\n\t"
      "v_cvt_pk_bf16_f32 v139, v142, v143\n\t"
      "buffer_store_dwordx4 v[136:139], v1, s[76:79], 0 offen offset:1024 sc1\n\t"
      "SK1_%=:\n\t"
      "v_mfma_f32_16x16x32_bf16 v[152:155], a[184:187], v[96:99], v[152:155]\n\t"
      "v_exp_f32 v108, v108\n\t"
      "v_mfma_f32_16x16x32_bf16 v[156:159], a[188:191], v[96:99], v[156:159]\n\t"
      "s_bitcmp1_b32 s80, 2\n\t"
      "s_cbranch_scc1 SK2_%=\n\t"
      "v_cvt_pk_bf16_f32 v144, v144, v145\n\t"
      "v_cvt_pk_bf16_f32 v145, v146, v147\n\t"
      "v_cvt_pk_bf16_f32 v146, v148, v149\n\t"
      "v_cvt_pk_bf16_f32 v147, v150, v151\n\t"
      "buffer_store_dwordx4 v[144:147], v1, s[76:79], 0 offen offset:2048 sc1\n\t"
      "SK2_%=:\n\t"
      "v_mfma_f32_16x16x32_bf16 v[192:195], a[128:131], v[80:83], v[192:195]\n\t"
      "v_cvt_pk_bf16_f32 v101, v106, v107\n\t"
      "v_mfma_f32_16x16x32_bf16 v[196:199], a[132:135], v[80:83], v[196:199]\n\t"
      "s_bitcmp1_b32 s80, 3\n\t"
      "s_cbranch_scc1 SK3_%=\n\t"
      "v_cvt_pk_bf16_f32 v152, v152, v153\n\t"
      "v_cvt_pk_bf16_f32 v153, v154, v155\n\t"
      "v_cvt_pk_bf16_f32 v154, v156, v157\n\t"
      "v_cvt_pk_bf16_f32 v155, v158, v159\n\t"
      "buffer_store_dwordx4 v[152:155], v1, s[76:79], 0 offen offset:3072 sc1\n\t"
      "SK3_%=:\n\t"
      "v_mfma_f32_16x16x32_bf16 v[200:203], a[136:139], v[80:83], v[200:203]\n\t"
      "v_exp_f32 v109, v109\n\t"
      "v_mfma_f32_16x16x32_bf16 v[204:207], a[140:143], v[80:83], v[204:207]\n\t"
      "v_exp_f32 v110, v110\n\t"
      "v_mfma_f32_16x16x32_bf16 v[208:211], a[144:147], v[80:83], v[208:211]\n\t"
      "v_cvt_pk_bf16_f32 v102, v108, v109\n\t"
      "v_mfma_f32_16x16x32_bf16 v[212:215], a[148:151], v[80:83], v[212:215]\n\t"
      "v_exp_f32 v111, v111\n\t"
      "v_mfma_f32_16x16x32_bf16 v[216:219], a[152:155], v[80:83], v[216:219]\n\t"
      "v_exp_f32 v112, v112\n\t"
      "v_cvt_pk_bf16_f32 v103, v110, v111\n\t"
      "v_mfma_f32_16x16x32_bf16 v[220:223], a[156:159], v[80:83], v[220:223]\n\t"
      "v_exp_f32 v113, v113\n\t"
      "v_mfma_f32_16x16x32_bf16 v[160:163], a[160:163], v[100:103], v[160:163]\n\t"
      "v_exp_f32 v114, v114\n\t"
      "v_mfma_f32_16x16x32_bf16 v[164:167], a[164:167], v[100:103], v[164:167]\n\t"
      "v_cvt_pk_bf16_f32 v112, v112, v113\n\t"
      "v_mfma_f32_16x16x32_bf16 v[168:171], a[168:171], v[100:103], v[168:171]\n\t"
      "v_exp_f32 v115, v115\n\t"
      "v_mfma_f32_16x16x32_bf16 v[172:175], a[172:175], v[100:103], v[172:175]\n\t"
      "s_bitcmp1_b32 s80, 4\n\t"
      "s_cbranch_scc1 SK4_%=\n\t"
      "v_cvt_pk_bf16_f32 v160, v160, v161\n\t"
      "v_cvt_pk_bf16_f32 v161, v162, v163\n\t"
      "v_cvt_pk_bf16_f32 v162, v164, v165\n\t"
      "v_cvt_pk_bf16_f32 v163, v166, v167\n\t"
      "buffer_store_dwordx4 v[160:163], v1, s[76:79], s55 offen offset:0 sc1\n\t"
      "SK4_%=:\n\t"
      "v_mfma_f32_16x16x32_bf16 v[176:179], a[176:179], v[100:103], v[176:179]\n\t"
      "v_exp_f32 v116, v116\n\t"
      "v_mfma_f32_16x16x32_bf16 v[180:183], a[180:183], v[100:103], v[180:183]\n\t"
      "s_bitcmp1_b32 s80, 5\n\t"
      "s_cbranch_scc1 SK5_%=\n\t"
      "v_cvt_pk_bf16_f32 v168, v168, v169\n\t"
      "v_cvt_pk_bf16_f32 v169, v170, v171\n\t"
      "v_cvt_pk_bf16_f32 v170, v172, v173\n\t"
      "v_cvt_pk_bf16_f32 v171, v174, v175\n\t"
      "buffer_store_dwordx4 v[168:171], v1, s[76:79], s55 offen offset:1024 sc1\n\t"
      "SK5_%=:\n\t"
      "v_mfma_f32_16x16x32_bf16 v[184:187], a[184:187], v[100:103], v[184:187]\n\t"
      "v_cvt_pk_bf16_f32 v113, v114, v115\n\t"
      "v_mfma_f32_16x16x32_bf16 v[188:191], a[188:191], v[100:103], v[188:191]\n\t"
      "s_bitcmp1_b32 s80, 6\n\t"
      "s_cbranch_scc1 SK6_%=\n\t"
      "v_cvt_pk_bf16_f32 v176, v176, v177\n\t"
      "v_cvt_pk_bf16_f32 v177, v178, v179\n\t"
      "v_cvt_pk_bf16_f32 v178, v180, v181\n\t"
      "v_cvt_pk_bf16_f32 v179, v182, v183\n\t"
      "buffer_store_dwordx4 v[176:179], v1, s[76:79], s55 offen offset:2048 sc1\n\t"
      "SK6_%=:\n\t"
      "v_smfmac_f32_16x16x64_bf16 v[48:51], a[192:195], v[96:103], v6\n\t"
      "v_exp_f32 v117, v117\n\t"
      "v_mfma_f32_16x16x32_bf16 v[224:227], a[128:131], v[84:87], v[224:227]\n\t"
      "s_bitcmp1_b32 s80, 7\n\t"
      "s_cbranch_scc1 SK7_%=\n\t"
      "v_cvt_pk_bf16_f32 v184, v184, v185\n\t"
      "v_cvt_pk_bf16_f32 v185, v186, v187\n\t"
      "v_cvt_pk_bf16_f32 v186, v188, v189\n\t"
      "v_cvt_pk_bf16_f32 v187, v190, v191\n\t"
      "buffer_store_dwordx4 v[184:187], v1, s[76:79], s55 offen offset:3072 sc1\n\t"
      "SK7_%=:\n\t"
      "v_mfma_f32_16x16x32_bf16 v[228:231], a[132:135], v[84:87], v[228:231]\n\t"
      "v_exp_f32 v118, v118\n\t"
      "v_mfma_f32_16x16x32_bf16 v[232:235], a[136:139], v[84:87], v[232:235]\n\t"
      "v_cvt_pk_bf16_f32 v114, v116, v117\n\t"
      "v_mfma_f32_16x16x32_bf16 v[236:239], a[140:143], v[84:87], v[236:239]\n\t"
      "v_exp_f32 v119, v119\n\t"
      "v_mfma_f32_16x16x32_bf16 v[240:243], a[144:147], v[84:87], v[240:243]\n\t"
      "v_exp_f32 v120, v120\n\t"
      "v_mfma_f32_16x16x32_bf16 v[244:247], a[148:151], v[84:87], v[244:247]\n\t"
      "v_cvt_pk_bf16_f32 v115, v118, v119\n\t"
      "v_mfma_f32_16x16x32_bf16 v[248:251], a[152:155], v[84:87], v[248:251]\n\t"
      "v_exp_f32 v121, v121\n\t"
      "v_mfma_f32_16x16x32_bf16 v[252:255], a[156:159], v[84:87], v[252:255]\n\t"
      "v_exp_f32 v122, v122\n\t"
      "v_smfmac_f32_16x16x64_bf16 v[48:51], a[196:199], v[80:87], v6\n\t"
      "v_cvt_pk_bf16_f32 v116, v120, v121\n\t"
      "v_mfma_f32_16x16x32_bf16 v[192:195], a[160:163], v[112:115], v[192:195]\n\t"
      "v_exp_f32 v123, v123\n\t"
      "v_mfma_f32_16x16x32_bf16 v[196:199], a[164:167], v[112:115], v[196:199]\n\t"
      "v_exp_f32 v124, v124\n\t"
      "v_mfma_f32_16x16x32_bf16 v[200:203], a[168:171], v[112:115], v[200:203]\n\t"
      "v_cvt_pk_bf16_f32 v117, v122, v123\n\t"
      "v_mfma_f32_16x16x32_bf16 v[204:207], a[172:175], v[112:115], v[204:207]\n\t"
      "v_exp_f32 v125, v125\n\t"
      "v_mfma_f32_16x16x32_bf16 v[208:211], a[176:179], v[112:115], v[208:211]\n\t"
      "v_exp_f32 v126, v126\n\t"
      "v_mfma_f32_16x16x32_bf16 v[212:215], a[180:183], v[112:115], v[212:215]\n\t"
      "v_cvt_pk_bf16_f32 v118, v124, v125\n\t"
      "v_exp_f32 v127, v127\n\t"
      "v_mfma_f32_16x16x32_bf16 v[216:219], a[184:187], v[112:115], v[216:219]\n\t"
      "v_cvt_pk_bf16_f32 v119, v126, v127\n\t"
      "v_mfma_f32_16x16x32_bf16 v[220:223], a[188:191], v[112:115], v[220:223]\n\t"
      "s_bitcmp1_b32 s80, 8\n\t"
      "s_cbranch_scc1 SK8_%=\n\t"
      "v_cvt_pk_bf16_f32 v192, v192, v193\n\t"
      "v_cvt_pk_bf16_f32 v193, v194, v195\n\t"
      "v_cvt_pk_bf16_f32 v194, v196, v197\n\t"
      "v_cvt_pk_bf16_f32 v195, v198, v199\n\t"
      "buffer_store_dwordx4 v[192:195], v1, s[76:79], s56 offen offset:0 sc1\n\t"
      "SK8_%=:\n\t"
      "v_smfmac_f32_16x16x64_bf16 v[48:51], a[196:199], v[112:119], v6\n\t"
      "s_bitcmp1_b32 s80, 9\n\t"
      "s_cbranch_scc1 SK9_%=\n\t"
      "v_cvt_pk_bf16_f32 v200, v200, v201\n\t"
      "v_cvt_pk_bf16_f32 v201, v202, v203\n\t"
      "v_cvt_pk_bf16_f32 v202, v204, v205\n\t"
      "v_cvt_pk_bf16_f32 v203, v206, v207\n\t"
      "buffer_store_dwordx4 v[200:203], v1, s[76:79], s56 offen offset:1024 sc1\n\t"
      "SK9_%=:\n\t"
      "v_mfma_f32_16x16x32_bf16 v[224:227], a[160:163], v[116:119], v[224:227]\n\t"
      "s_bitcmp1_b32 s80, 10\n\t"
      "s_cbranch_scc1 SK10_%=\n\t"
      "v_cvt_pk_bf16_f32 v208, v208, v209\n\t"
      "v_cvt_pk_bf16_f32 v209, v210, v211\n\t"
      "v_cvt_pk_bf16_f32 v210, v212, v213\n\t"
      "v_cvt_pk_bf16_f32 v211, v214, v215\n\t"
      "buffer_store_dwordx4 v[208:211], v1, s[76:79], s56 offen offset:2048 sc1\n\t"
      "SK10_%=:\n\t"
      "v_mfma_f32_16x16x32_bf16 v[228:231], a[164:167], v[116:119], v[228:231]\n\t"
      "v_lshrrev_b32 v26, 1, v1\n\t"
      "v_and_b32 v28, 0x78, v26\n\t"
      "v_add_u32 v26, 0x4000, v26\n\t"
      "v_add_f32 v17, v48, v49\n\t"
      "v_cmp_gt_f32_e32 vcc, 0x7149f2ca, v17\n\t"
      "s_waitcnt vmcnt(16)\n\t"
      "v_readfirstlane_b32 %[oe0], v22\n\t"
      "v_readfirstlane_b32 %[oe1], v23\n\t"
      "v_cndmask_b32_e32 v17, -1, v17, vcc\n\t"
      "buffer_store_dwordx2 v[16:17], v26, s[76:79], 0 offen sc1\n\t"
      "v_mfma_f32_16x16x32_bf16 v[232:235], a[168:171], v[116:119], v[232:235]\n\t"
      "s_bitcmp1_b32 s80, 11\n\t"
      "s_cbranch_scc1 SK11_%=\n\t"
      "v_cvt_pk_bf16_f32 v216, v216, v217\n\t"
      "v_cvt_pk_bf16_f32 v217, v218, v219\n\t"
      "v_cvt_pk_bf16_f32 v218, v220, v221\n\t"
      "v_cvt_pk_bf16_f32 v219, v222, v223\n\t"
      "buffer_store_dwordx4 v[216:219], v1, s[76:79], s56 offen offset:3072 sc1\n\t"
      "SK11_%=:\n\t"
      "v_mfma_f32_16x16x32_bf16 v[236:239], a[172:175], v[116:119], v[236:239]\n\t"
      "s_bitcmp1_b32 s80, 12\n\t"
      "s_cbranch_scc1 SK12_%=\n\t"
      "v_cvt_pk_bf16_f32 v224, v224, v225\n\t"
      "v_cvt_pk_bf16_f32 v225, v226, v227\n\t"
      "v_cvt_pk_bf16_f32 v226, v228, v229\n\t"
      "v_cvt_pk_bf16_f32 v227, v230, v231\n\t"
      "buffer_store_dwordx4 v[224:227], v1, s[76:79], s57 offen offset:0 sc1\n\t"
      "SK12_%=:\n\t"
      "v_mfma_f32_16x16x32_bf16 v[240:243], a[176:179], v[116:119], v[240:243]\n\t"
      "v_mfma_f32_16x16x32_bf16 v[244:247], a[180:183], v[116:119], v[244:247]\n\t"
      "s_bitcmp1_b32 s80, 13\n\t"
      "s_cbranch_scc1 SK13_%=\n\t"
      "v_cvt_pk_bf16_f32 v232, v232, v233\n\t"
      "v_cvt_pk_bf16_f32 v233, v234, v235\n\t"
      "v_cvt_pk_bf16_f32 v234, v236, v237\n\t"
      "v_cvt_pk_bf16_f32 v235, v238, v239\n\t"
      "buffer_store_dwordx4 v[232:235], v1, s[76:79], s57 offen offset:1024 sc1\n\t"
      "SK13_%=:\n\t"
      "v_mfma_f32_16x16x32_bf16 v[248:251], a[184:187], v[116:119], v[248:251]\n\t"
      "v_mfma_f32_16x16x32_bf16 v[252:255], a[188:191], v[116:119], v[252:255]\n\t"
      "s_nop 0\n\t"
      "s_bitcmp1_b32 s80, 14\n\t"
      "s_cbranch_scc1 SK14_%=\n\t"
      "v_cvt_pk_bf16_f32 v240, v240, v241\n\t"
      "v_cvt_pk_bf16_f32 v241, v242, v243\n\t"
      "v_cvt_pk_bf16_f32 v242, v244, v245\n\t"
      "v_cvt_pk_bf16_f32 v243, v246, v247\n\t"
      "buffer_store_dwordx4 v[240:243], v1, s[76:79], s57 offen offset:2048 sc1\n\t"
      "SK14_%=:\n\t"
      "s_nop 1\n\t"
      "s_bitcmp1_b32 s80, 15\n\t"
      "s_cbranch_scc1 SK15_%=\n\t"
      "v_cvt_pk_bf16_f32 v248, v248, v249\n\t"
      "v_cvt_pk_bf16_f32 v249, v250, v251\n\t"
      "v_cvt_pk_bf16_f32 v250, v252, v253\n\t"
      "v_cvt_pk_bf16_f32 v251, v254, v255\n\t"
      "buffer_store_dwordx4 v[248:251], v1, s[76:79], s57 offen offset:3072 sc1\n\t"
      "SK15_%=:\n\t"
      "global_load_dwordx2 v[20:21], v28, s[84:85] offset:512 sc1\n\t"
      "s_add_u32 s68, %[oe0], 0x7f4a7c15\n\t"
      "s_addc_u32 s69, %[oe1], 0x9e3779b9\n\t"
      "v_mov_b32 v22, s68\n\t"
      "v_mov_b32 v23, s69\n\t"
      "s_ff1_i32_b32 s48, s80\n\t"
      "s_bcnt1_i32_b32 s47, s80\n\t"
      "s_lshr_b32 s46, s48, 2\n\t"
      "s_lshl_b32 s46, s46, 6\n\t"
      "v_lshrrev_b32 v14, 3, v1\n\t"
      "v_and_b32 v14, 64, v14\n\t"
      "v_lshrrev_b32 v13, 1, v28\n\t"
      "v_add3_u32 v14, v14, v13, s46\n\t"
      "ds_bpermute_b32 v18, v14, v16\n\t"
      "ds_bpermute_b32 v19, v14, v17\n\t"
      "v_lshlrev_b32 v13, 1, v14\n\t"
      "v_add_u32 v13, 0x4000, v13\n\t"
      "s_mul_i32 s46, %[oj], 69632\n\t"
      "s_sub_u32 s36, s76, s46\n\t"
      "s_subb_u32 s37, s77, 0\n\t"
      "s_mul_i32 s38, %[on], 69632\n\t"
      "s_mov_b32 s39, 0x20000\n\t"
      "s_mov_b32 s72, s82\n\t"
      "s_mov_b32 s73, s83\n\t"
      "s_movk_i32 s74, 0x4000\n\t"
      "s_mov_b32 s75, 0x20000\n\t"
      "s_cmp_ge_u32 %[on], 6\n\t"
      "s_cselect_b32 s81, 3, 4\n\t"
      "s_cmp_ge_u32 %[on], 8\n\t"
      "s_cselect_b32 s81, 2, s81\n\t"
      "s_bfm_b32 s41, %[on], 0\n\t"
      "s_bitset0_b32 s41, %[oj]\n\t"
      "s_and_b32 s46, %[oslot], 1\n\t"
      "s_lshr_b32 s46, 0xaaaa, s46\n\t"
      "s_andn2_b32 s70, s41, s46\n\t"
      "s_and_b32 s46, s41, s46\n\t"
      "s_pack_ll_b32_b16 s58, s46, s70\n\t"
      "s_flbit_i32_b32 s70, s58\n\t"
      "s_xor_b32 s70, s70, 31\n\t"
      "s_and_b32 s44, s70, 15\n\t"
      "s_mov_b32 s43, 0\n\t"
      "s_mov_b32 s54, 1048576\n\t"
      "s_mov_b32 s59, 0xffff0000\n\t"
      "s_lshl_b32 s46, s48, 10\n\t"
      "v_add_u32 v24, s46, v1\n\t"
      "s_cmp_eq_u32 s47, s81\n\t"
      "s_cselect_b32 s46, 0, 0x80000000\n\t"
      "v_or_b32 v25, s46, v24\n\t"
      "v_lshlrev_b32 v27, 5, v28\n\t"
      "v_lshrrev_b32 v29, 3, v1\n\t"
      "v_and_or_b32 v27, v29, 32, v27\n\t"
      "v_lshrrev_b32 v29, 5, v1\n\t"
      "v_and_or_b32 v27, v29, 16, v27\n\t"
      "s_lshl_b32 s46, %[oj], 3\n\t"
      "v_mov_b32 v29, s46\n\t"
      "s_and_b32 s45, s48, 3\n\t"
      "s_cmp_ge_u32 s45, 4\n\t"
      "s_cselect_b64 s[60:61], -1, 0\n\t"
      "s_add_u32 s46, s48, 0\n\t"
      "s_mul_i32 s50, s46, 0x440\n\t"
      "s_and_b32 s50, s50, 0x30c0\n\t"
      "s_cmp_ge_u32 s45, 3\n\t"
      "s_cselect_b64 s[62:63], -1, 0\n\t"
      "s_add_u32 s46, s48, 1\n\t"
      "s_mul_i32 s51, s46, 0x440\n\t"
      "s_and_b32 s51, s51, 0x30c0\n\t"
      "s_cmp_ge_u32 s45, 2\n\t"
      "s_cselect_b64 s[64:65], -1, 0\n\t"
      "s_add_u32 s46, s48, 2\n\t"
      "s_mul_i32 s52, s46, 0x440\n\t"
      "s_and_b32 s52, s52, 0x30c0\n\t"
      "s_cmp_ge_u32 s45, 1\n\t"
      "s_cselect_b64 s[66:67], -1, 0\n\t"
      "s_add_u32 s46, s48, 3\n\t"
      "s_mul_i32 s53, s46, 0x440\n\t"
      "s_and_b32 s53, s53, 0x30c0\n\t"
      "s_lshl_b32 s45, s48, 3\n\t"
      "s_set_gpr_idx_on s45, gpr_idx(SRC0)\n\t"
      "s_nop 0\n\t"
      "v_mov_b32 v32, v128\n\t"
      "v_mov_b32 v33, v129\n\t"
      "v_mov_b32 v34, v130\n\t"
      "v_mov_b32 v35, v131\n\t"
      "v_mov_b32 v36, v132\n\t"
      "v_mov_b32 v37, v133\n\t"
      "v_mov_b32 v38, v134\n\t"
      "v_mov_b32 v39, v135\n\t"
      "v_mov_b32 v40, v136\n\t"
      "v_mov_b32 v41, v137\n\t"
      "v_mov_b32 v42, v138\n\t"
      "v_mov_b32 v43, v139\n\t"
      "v_mov_b32 v44, v140\n\t"
      "v_mov_b32 v45, v141\n\t"
      "v_mov_b32 v46, v142\n\t"
      "v_mov_b32 v47, v143\n\t"
      "s_cmp_eq_u32 s81, 2\n\t"
      "s_cbranch_scc1 OC_%=\n\t"
      "v_mov_b32 v48, v144\n\t"
      "v_mov_b32 v49, v145\n\t"
      "v_mov_b32 v50, v146\n\t"
      "v_mov_b32 v51, v147\n\t"
      "v_mov_b32 v52, v148\n\t"
      "v_mov_b32 v53, v149\n\t"
      "v_mov_b32 v54, v150\n\t"
      "v_mov_b32 v55, v151\n\t"
      "s_cmp_eq_u32 s81, 3\n\t"
      "s_cbranch_scc1 OC_%=\n\t"
      "v_mov_b32 v56, v152\n\t"
      "v_mov_b32 v57, v153\n\t"
      "v_mov_b32 v58, v154\n\t"
      "v_mov_b32 v59, v155\n\t"
      "v_mov_b32 v60, v156\n\t"
      "v_mov_b32 v61, v157\n\t"
      "v_mov_b32 v62, v158\n\t"
      "v_mov_b32 v63, v159\n\t"
      "OC_%=:\n\t"
      "s_set_gpr_idx_off\n\t"
      "s_cmp_eq_u32 s80, 0\n\t"
      "s_waitcnt vmcnt(0) lgkmcnt(0)\n\t"
      "s_barrier\n\t"
      "s_cbranch_scc1 GEN_%=\n\t"
      "s_cmp_lg_u32 s49, 0\n\t"
      "s_cbranch_scc1 NOFL_%=\n\t"
      "s_mov_b64 exec, 1\n\t"
      "global_store_dwordx2 v29, v[22:23], s[84:85] offset:512 sc1\n\t"
      "s_mov_b64 exec, -1\n\t"
      "NOFL_%=:\n\t"
      "s_cmp_eq_u32 s81, 3\n\t"
      "s_cbranch_scc1 P3_%=\n\t"
      "s_cmp_eq_u32 s81, 4\n\t"
      "s_cbranch_scc1 P4_%=\n\t"
      "P2_%=:\n\t"
      "v_cmp_eq_u64_e64 vcc, v[20:21], s[68:69]\n\t"
      "s_and_b32 s42, vcc_lo, s41\n\t"
      "s_cbranch_scc0 NL2_%=\n\t"
      "PN2_%=:\n\t"
      "s_andn2_b32 s41, s41, s42\n\t"
      "s_and_b32 s46, s42, 0xf\n\t"
      "s_cbranch_scc0 G0_2_%=\n\t"
      "s_bitcmp1_b32 s42, 0\n\t"
      "s_cbranch_scc0 C0_2_%=\n\t"
      "buffer_load_dwordx2 v[64:65], v13, s[36:39], 0 offen sc1\n\t"
      "buffer_load_dwordx4 v[66:69], v24, s[36:39], 0 offen offset:0 sc1\n\t"
      "buffer_load_dwordx4 v[70:73], v25, s[36:39], 0 offen offset:1024 sc1\n\t"
      "C0_2_%=:\n\t"
      "s_bitcmp1_b32 s42, 1\n\t"
      "s_cbranch_scc0 C1_2_%=\n\t"
      "s_mov_b32 s46, 69632\n\t"
      "buffer_load_dwordx2 v[74:75], v13, s[36:39], s46 offen sc1\n\t"
      "buffer_load_dwordx4 v[76:79], v24, s[36:39], s46 offen offset:0 sc1\n\t"
      "buffer_load_dwordx4 v[80:83], v25, s[36:39], s46 offen offset:1024 sc1\n\t"
      "C1_2_%=:\n\t"
      "s_bitcmp1_b32 s42, 2\n\t"
      "s_cbranch_scc0 C2_2_%=\n\t"
      "s_mov_b32 s46, 139264\n\t"
      "buffer_load_dwordx2 v[84:85], v13, s[36:39], s46 offen sc1\n\t"
      "buffer_load_dwordx4 v[86:89], v24, s[36:39], s46 offen offset:0 sc1\n\t"
      "buffer_load_dwordx4 v[90:93], v25, s[36:39], s46 offen offset:1024 sc1\n\t"
      "C2_2_%=:\n\t"
      "s_bitcmp1_b32 s42, 3\n\t"
      "s_cbranch_scc0 C3_2_%=\n\t"
      "s_mov_b32 s46, 208896\n\t"
      "buffer_load_dwordx2 v[94:95], v13, s[36:39], s46 offen sc1\n\t"
      "buffer_load_dwordx4 v[96:99], v24, s[36:39], s46 offen offset:0 sc1\n\t"
      "buffer_load_dwordx4 v[100:103], v25, s[36:39], s46 offen offset:1024 sc1\n\t"
      "C3_2_%=:\n\t"
      "G0_2_%=:\n\t"
      "s_and_b32 s46, s42, 0xf0\n\t"
      "s_cbranch_scc0 G4_2_%=\n\t"
      "s_bitcmp1_b32 s42, 4\n\t"
      "s_cbranch_scc0 C4_2_%=\n\t"
      "s_mov_b32 s46, 278528\n\t"
      "buffer_load_dwordx2 v[104:105], v13, s[36:39], s46 offen sc1\n\t"
      "buffer_load_dwordx4 v[106:109], v24, s[36:39], s46 offen offset:0 sc1\n\t"
      "buffer_load_dwordx4 v[110:113], v25, s[36:39], s46 offen offset:1024 sc1\n\t"
      "C4_2_%=:\n\t"
      "s_bitcmp1_b32 s42, 5\n\t"
      "s_cbranch_scc0 C5_2_%=\n\t"
      "s_mov_b32 s46, 348160\n\t"
      "buffer_load_dwordx2 v[114:115], v13, s[36:39], s46 offen sc1\n\t"
      "buffer_load_dwordx4 v[116:119], v24, s[36:39], s46 offen offset:0 sc1\n\t"
      "buffer_load_dwordx4 v[120:123], v25, s[36:39], s46 offen offset:1024 sc1\n\t"
      "C5_2_%=:\n\t"
      "s_bitcmp1_b32 s42, 6\n\t"
      "s_cbranch_scc0 C6_2_%=\n\t"
      "s_mov_b32 s46, 417792\n\t"
      "buffer_load_dwordx2 v[124:125], v13, s[36:39], s46 offen sc1\n\t"
      "buffer_load_dwordx4 v[126:129], v24, s[36:39], s46 offen offset:0 sc1\n\t"
      "buffer_load_dwordx4 v[130:133], v25, s[36:39], s46 offen offset:1024 sc1\n\t"
      "C6_2_%=:\n\t"
      "s_bitcmp1_b32 s42, 7\n\t"
      "s_cbranch_scc0 C7_2_%=\n\t"
      "s_mov_b32 s46, 487424\n\t"
      "buffer_load_dwordx2 v[134:135], v13, s[36:39], s46 offen sc1\n\t"
      "buffer_load_dwordx4 v[136:139], v24, s[36:39], s46 offen offset:0 sc1\n\t"
      "buffer_load_dwordx4 v[140:143], v25, s[36:39], s46 offen offset:1024 sc1\n\t"
      "C7_2_%=:\n\t"
      "G4_2_%=:\n\t"
      "s_and_b32 s46, s42, 0xf00\n\t"
      "s_cbranch_scc0 G8_2_%=\n\t"
      "s_bitcmp1_b32 s42, 8\n\t"
      "s_cbranch_scc0 C8_2_%=\n\t"
      "s_mov_b32 s46, 557056\n\t"
      "buffer_load_dwordx2 v[144:145], v13, s[36:39], s46 offen sc1\n\t"
      "buffer_load_dwordx4 v[146:149], v24, s[36:39], s46 offen offset:0 sc1\n\t"
      "buffer_load_dwordx4 v[150:153], v25, s[36:39], s46 offen offset:1024 sc1\n\t"
      "C8_2_%=:\n\t"
      "s_bitcmp1_b32 s42, 9\n\t"
      "s_cbranch_scc0 C9_2_%=\n\t"
      "s_mov_b32 s46, 626688\n\t"
      "buffer_load_dwordx2 v[154:155], v13, s[36:39], s46 offen sc1\n\t"
      "buffer_load_dwordx4 v[156:159], v24, s[36:39], s46 offen offset:0 sc1\n\t"
      "buffer_load_dwordx4 v[160:163], v25, s[36:39], s46 offen offset:1024 sc1\n\t"
      "C9_2_%=:\n\t"
      "s_bitcmp1_b32 s42, 10\n\t"
      "s_cbranch_scc0 C10_2_%=\n\t"
      "s_mov_b32 s46, 696320\n\t"
      "buffer_load_dwordx2 v[164:165], v13, s[36:39], s46 offen sc1\n\t"
      "buffer_load_dwordx4 v[166:169], v24, s[36:39], s46 offen offset:0 sc1\n\t"
      "buffer_load_dwordx4 v[170:173], v25, s[36:39], s46 offen offset:1024 sc1\n\t"
      "C10_2_%=:\n\t"
      "s_bitcmp1_b32 s42, 11\n\t"
      "s_cbranch_scc0 C11_2_%=\n\t"
      "s_mov_b32 s46, 765952\n\t"
      "buffer_load_dwordx2 v[174:175], v13, s[36:39], s46 offen sc1\n\t"
      "buffer_load_dwordx4 v[176:179], v24, s[36:39], s46 offen offset:0 sc1\n\t"
      "buffer_load_dwordx4 v[180:183], v25, s[36:39], s46 offen offset:1024 sc1\n\t"
      "C11_2_%=:\n\t"
      "G8_2_%=:\n\t"
      "s_and_b32 s46, s42, 0xf000\n\t"
      "s_cbranch_scc0 G12_2_%=\n\t"
      "s_bitcmp1_b32 s42, 12\n\t"
      "s_cbranch_scc0 C12_2_%=\n\t"
      "s_mov_b32 s46, 835584\n\t"
      "buffer_load_dwordx2 v[184:185], v13, s[36:39], s46 offen sc1\n\t"
      "buffer_load_dwordx4 v[186:189], v24, s[36:39], s46 offen offset:0 sc1\n\t"
      "buffer_load_dwordx4 v[190:193], v25, s[36:39], s46 offen offset:1024 sc1\n\t"
      "C12_2_%=:\n\t"
      "s_bitcmp1_b32 s42, 13\n\t"
      "s_cbranch_scc0 C13_2_%=\n\t"
      "s_mov_b32 s46, 905216\n\t"
      "buffer_load_dwordx2 v[194:195], v13, s[36:39], s46 offen sc1\n\t"
      "buffer_load_dwordx4 v[196:199], v24, s[36:39], s46 offen offset:0 sc1\n\t"
      "buffer_load_dwordx4 v[200:203], v25, s[36:39], s46 offen offset:1024 sc1\n\t"
      "C13_2_%=:\n\t"
      "s_bitcmp1_b32 s42, 14\n\t"
      "s_cbranch_scc0 C14_2_%=\n\t"
      "s_mov_b32 s46, 974848\n\t"
      "buffer_load_dwordx2 v[204:205], v13, s[36:39], s46 offen sc1\n\t"
      "buffer_load_dwordx4 v[206:209], v24, s[36:39], s46 offen offset:0 sc1\n\t"
      "buffer_load_dwordx4 v[210:213], v25, s[36:39], s46 offen offset:1024 sc1\n\t"
      "C14_2_%=:\n\t"
      "s_bitcmp1_b32 s42, 15\n\t"
      "s_cbranch_scc0 C15_2_%=\n\t"
      "s_mov_b32 s46, 1044480\n\t"
      "buffer_load_dwordx2 v[214:215], v13, s[36:39], s46 offen sc1\n\t"
      "buffer_load_dwordx4 v[216:219], v24, s[36:39], s46 offen offset:0 sc1\n\t"
      "buffer_load_dwordx4 v[220:223], v25, s[36:39], s46 offen offset:1024 sc1\n\t"
      "C15_2_%=:\n\t"
      "G12_2_%=:\n\t"
      "NL2_%=:\n\t"
      "s_cmp_eq_u32 s41, 0\n\t"
      "s_cbranch_scc1 NP2_%=\n\t"
      "global_load_dwordx2 v[20:21], v28, s[84:85] offset:512 sc1\n\t"
      "NP2_%=:\n\t"
      "s_bitcmp1_b32 s43, s44\n\t"
      "s_cbranch_scc0 NB2_%=\n\t"
      "B2_%=:\n\t"
      "s_mul_i32 s45, s44, 10\n\t"
      "s_bitset0_b32 s43, s44\n\t"
      "s_set_gpr_idx_on s45, gpr_idx(SRC1)\n\t"
      "s_bitset0_b32 s58, s70\n\t"
      "v_sub_f32 v12, v18, v64\n\t"
      "s_flbit_i32_b32 s70, s58\n\t"
      "v_exp_f32 v10, v12\n\t"
      "s_xor_b32 s70, s70, 31\n\t"
      "v_lshlrev_b32 v224, 16, v66\n\t"
      "s_and_b32 s44, s70, 15\n\t"
      "v_and_b32 v225, s59, v66\n\t"
      "v_lshlrev_b32 v226, 16, v67\n\t"
      "v_and_b32 v227, s59, v67\n\t"
      "v_lshlrev_b32 v228, 16, v68\n\t"
      "v_and_b32 v229, s59, v68\n\t"
      "v_lshlrev_b32 v230, 16, v69\n\t"
      "v_and_b32 v231, s59, v69\n\t"
      "v_fmac_f32 v19, v10, v65\n\t"
      "v_mov_b32 v11, v10\n\t"
      "v_lshlrev_b32 v232, 16, v70\n\t"
      "v_and_b32 v233, s59, v70\n\t"
      "v_lshlrev_b32 v234, 16, v71\n\t"
      "v_and_b32 v235, s59, v71\n\t"
      "v_lshlrev_b32 v236, 16, v72\n\t"
      "v_and_b32 v237, s59, v72\n\t"
      "v_lshlrev_b32 v238, 16, v73\n\t"
      "v_and_b32 v239, s59, v73\n\t"
      "v_permlane32_swap_b32 v10, v11\n\t"
      "s_set_gpr_idx_off\n\t"
      "s_bitcmp1_b32 s43, s44\n\t"
      "v_cndmask_b32_e64 v2, v10, v11, s[60:61]\n\t"
      "v_cndmask_b32_e64 v4, v10, v11, s[62:63]\n\t"
      "v_pk_fma_f32 v[32:33], v[224:225], v[2:3], v[32:33] op_sel_hi:[1,0,1]\n\t"
      "v_pk_fma_f32 v[34:35], v[226:227], v[2:3], v[34:35] op_sel_hi:[1,0,1]\n\t"
      "v_pk_fma_f32 v[36:37], v[228:229], v[2:3], v[36:37] op_sel_hi:[1,0,1]\n\t"
      "v_pk_fma_f32 v[38:39], v[230:231], v[2:3], v[38:39] op_sel_hi:[1,0,1]\n\t"
      "v_pk_fma_f32 v[40:41], v[232:233], v[4:5], v[40:41] op_sel_hi:[1,0,1]\n\t"
      "v_pk_fma_f32 v[42:43], v[234:235], v[4:5], v[42:43] op_sel_hi:[1,0,1]\n\t"
      "v_pk_fma_f32 v[44:45], v[236:237], v[4:5], v[44:45] op_sel_hi:[1,0,1]\n\t"
      "v_pk_fma_f32 v[46:47], v[238:239], v[4:5], v[46:47] op_sel_hi:[1,0,1]\n\t"
      "s_cbranch_scc1 B2_%=\n\t"
      "NB2_%=:\n\t"
      "s_cmp_eq_u32 s58, 0\n\t"
      "s_cbranch_scc1 FA_%=\n\t"
      "s_or_b32 s43, s43, s42\n\t"
      "s_waitcnt vmcnt(0)\n\t"
      "s_sub_u32 s54, s54, 1\n\t"
      "s_cbranch_scc0 P2_%=\n\t"
      "s_branch TMO_%=\n\t"
      "FA_%=:\n\t"
      "s_cmp_lg_u32 s49, 0\n\t"
      "v_rcp_f32 v10, v19\n\t"
      "s_cbranch_scc1 FB_%=\n\t"
      "s_mov_b64 exec, 1\n\t"
      "v_mov_b32 v29, 0\n\t"
      "global_store_dwordx2 v29, v[22:23], s[84:85] sc1\n\t"
      "s_mov_b64 exec, -1\n\t"
      "FB_%=:\n\t"
      "s_mov_b32 s46, 0x7149f2ca\n\t"
      "s_mov_b32 s44, 0x4e6e6b28\n\t"
      "v_cmp_gt_f32_e64 s[38:39], s46, v19\n\t"
      "v_cmp_gt_f32_e64 vcc, s44, v18\n\t"
      "s_movk_i32 s46, 0x207\n\t"
      "s_and_b64 vcc, vcc, s[38:39]\n\t"
      "v_cndmask_b32_e32 v10, -1, v10, vcc\n\t"
      "v_mov_b32 v11, v10\n\t"
      "s_mov_b32 %[ob1], 0\n\t"
      "s_mov_b32 %[ob2], 0\n\t"
      "v_permlane32_swap_b32 v10, v11\n\t"
      "s_mov_b32 %[ob3], 0\n\t"
      "s_mov_b32 s42, 0\n\t"
      "v_cndmask_b32_e64 v2, v10, v11, s[60:61]\n\t"
      "v_pk_mul_f32 v[32:33], v[32:33], v[2:3] op_sel_hi:[1,0]\n\t"
      "v_pk_mul_f32 v[34:35], v[34:35], v[2:3] op_sel_hi:[1,0]\n\t"
      "v_pk_mul_f32 v[36:37], v[36:37], v[2:3] op_sel_hi:[1,0]\n\t"
      "v_pk_mul_f32 v[38:39], v[38:39], v[2:3] op_sel_hi:[1,0]\n\t"
      "v_pk_add_f32 v[224:225], v[32:33], v[34:35]\n\t"
      "v_pk_add_f32 v[226:227], v[36:37], v[38:39]\n\t"
      "v_cvt_pk_bf16_f32 v32, v32, v33\n\t"
      "v_cvt_pk_bf16_f32 v33, v34, v35\n\t"
      "v_pk_add_f32 v[224:225], v[224:225], v[226:227]\n\t"
      "v_cvt_pk_bf16_f32 v34, v36, v37\n\t"
      "v_cvt_pk_bf16_f32 v35, v38, v39\n\t"
      "v_add_f32 v224, v224, v225\n\t"
      "v_cmp_class_f32_e64 vcc, v224, s46\n\t"
      "v_permlane16_swap_b32 v32, v34\n\t"
      "v_permlane16_swap_b32 v33, v35\n\t"
      "s_or_b32 s38, vcc_lo, vcc_hi\n\t"
      "s_lshr_b32 s39, s38, 16\n\t"
      "s_or_b32 s38, s38, s39\n\t"
      "s_pack_ll_b32_b16 s38, s38, s38\n\t"
      "s_mov_b32 s39, s38\n\t"
      "s_not_b64 exec, s[38:39]\n\t"
      "buffer_store_dwordx4 v[32:35], v27, s[72:75], s50 offen\n\t"
      "s_mov_b64 exec, -1\n\t"
      "s_mov_b32 %[ob0], s38\n\t"
      "s_or_b32 s42, s42, s38\n\t"
      "s_cmp_le_u32 s47, 1\n\t"
      "s_cbranch_scc1 FE_%=\n\t"
      "v_cndmask_b32_e64 v2, v10, v11, s[62:63]\n\t"
      "v_pk_mul_f32 v[40:41], v[40:41], v[2:3] op_sel_hi:[1,0]\n\t"
      "v_pk_mul_f32 v[42:43], v[42:43], v[2:3] op_sel_hi:[1,0]\n\t"
      "v_pk_mul_f32 v[44:45], v[44:45], v[2:3] op_sel_hi:[1,0]\n\t"
      "v_pk_mul_f32 v[46:47], v[46:47], v[2:3] op_sel_hi:[1,0]\n\t"
      "v_pk_add_f32 v[224:225], v[40:41], v[42:43]\n\t"
      "v_pk_add_f32 v[226:227], v[44:45], v[46:47]\n\t"
      "v_cvt_pk_bf16_f32 v40, v40, v41\n\t"
      "v_cvt_pk_bf16_f32 v41, v42, v43\n\t"
      "v_pk_add_f32 v[224:225], v[224:225], v[226:227]\n\t"
      "v_cvt_pk_bf16_f32 v42, v44, v45\n\t"
      "v_cvt_pk_bf16_f32 v43, v46, v47\n\t"
      "v_add_f32 v224, v224, v225\n\t"
      "v_cmp_class_f32_e64 vcc, v224, s46\n\t"
      "v_permlane16_swap_b32 v40, v42\n\t"
      "v_permlane16_swap_b32 v41, v43\n\t"
      "s_or_b32 s38, vcc_lo, vcc_hi\n\t"
      "s_lshr_b32 s39, s38, 16\n\t"
      "s_or_b32 s38, s38, s39\n\t"
      "s_pack_ll_b32_b16 s38, s38, s38\n\t"
      "s_mov_b32 s39, s38\n\t"
      "s_not_b64 exec, s[38:39]\n\t"
      "buffer_store_dwordx4 v[40:43], v27, s[72:75], s51 offen\n\t"
      "s_mov_b64 exec, -1\n\t"
      "s_mov_b32 %[ob1], s38\n\t"
      "s_or_b32 s42, s42, s38\n\t"
      "s_cmp_le_u32 s47, 2\n\t"
      "s_cbranch_scc1 FE_%=\n\t"
      "v_cndmask_b32_e64 v2, v10, v11, s[64:65]\n\t"
      "v_pk_mul_f32 v[48:49], v[48:49], v[2:3] op_sel_hi:[1,0]\n\t"
      "v_pk_mul_f32 v[50:51], v[50:51], v[2:3] op_sel_hi:[1,0]\n\t"
      "v_pk_mul_f32 v[52:53], v[52:53], v[2:3] op_sel_hi:[1,0]\n\t"
      "v_pk_mul_f32 v[54:55], v[54:55], v[2:3] op_sel_hi:[1,0]\n\t"
      "v_pk_add_f32 v[224:225], v[48:49], v[50:51]\n\t"
      "v_pk_add_f32 v[226:227], v[52:53], v[54:55]\n\t"
      "v_cvt_pk_bf16_f32 v48, v48, v49\n\t"
      "v_cvt_pk_bf16_f32 v49, v50, v51\n\t"
      "v_pk_add_f32 v[224:225], v[224:225], v[226:227]\n\t"
      "v_cvt_pk_bf16_f32 v50, v52, v53\n\t"
      "v_cvt_pk_bf16_f32 v51, v54, v55\n\t"
      "v_add_f32 v224, v224, v225\n\t"
      "v_cmp_class_f32_e64 vcc, v224, s46\n\t"
      "v_permlane16_swap_b32 v48, v50\n\t"
      "v_permlane16_swap_b32 v49, v51\n\t"
      "s_or_b32 s38, vcc_lo, vcc_hi\n\t"
      "s_lshr_b32 s39, s38, 16\n\t"
      "s_or_b32 s38, s38, s39\n\t"
      "s_pack_ll_b32_b16 s38, s38, s38\n\t"
      "s_mov_b32 s39, s38\n\t"
      "s_not_b64 exec, s[38:39]\n\t"
      "buffer_store_dwordx4 v[48:51], v27, s[72:75], s52 offen\n\t"
      "s_mov_b64 exec, -1\n\t"
      "s_mov_b32 %[ob2], s38\n\t"
      "s_or_b32 s42, s42, s38\n\t"
      "s_cmp_le_u32 s47, 3\n\t"
      "s_cbranch_scc1 FE_%=\n\t"
      "v_cndmask_b32_e64 v2, v10, v11, s[66:67]\n\t"
      "v_pk_mul_f32 v[56:57], v[56:57], v[2:3] op_sel_hi:[1,0]\n\t"
      "v_pk_mul_f32 v[58:59], v[58:59], v[2:3] op_sel_hi:[1,0]\n\t"
      "v_pk_mul_f32 v[60:61], v[60:61], v[2:3] op_sel_hi:[1,0]\n\t"
      "v_pk_mul_f32 v[62:63], v[62:63], v[2:3] op_sel_hi:[1,0]\n\t"
      "v_pk_add_f32 v[224:225], v[56:57], v[58:59]\n\t"
      "v_pk_add_f32 v[226:227], v[60:61], v[62:63]\n\t"
      "v_cvt_pk_bf16_f32 v56, v56, v57\n\t"
      "v_cvt_pk_bf16_f32 v57, v58, v59\n\t"
      "v_pk_add_f32 v[224:225], v[224:225], v[226:227]\n\t"
      "v_cvt_pk_bf16_f32 v58, v60, v61\n\t"
      "v_cvt_pk_bf16_f32 v59, v62, v63\n\t"
      "v_add_f32 v224, v224, v225\n\t"
      "v_cmp_class_f32_e64 vcc, v224, s46\n\t"
      "v_permlane16_swap_b32 v56, v58\n\t"
      "v_permlane16_swap_b32 v57, v59\n\t"
      "s_or_b32 s38, vcc_lo, vcc_hi\n\t"
      "s_lshr_b32 s39, s38, 16\n\t"
      "s_or_b32 s38, s38, s39\n\t"
      "s_pack_ll_b32_b16 s38, s38, s38\n\t"
      "s_mov_b32 s39, s38\n\t"
      "s_not_b64 exec, s[38:39]\n\t"
      "buffer_store_dwordx4 v[56:59], v27, s[72:75], s53 offen\n\t"
      "s_mov_b64 exec, -1\n\t"
      "s_mov_b32 %[ob3], s38\n\t"
      "s_or_b32 s42, s42, s38\n\t"
      "FE_%=:\n\t"
      "s_cmp_eq_u32 s42, 0\n\t"
      "s_cbranch_scc1 IDLE2_%=\n\t"
      "s_mov_b32 %[omode], s48\n\t"
      "s_branch END_%=\n\t"
      "IDLE_%=:\n\t"
      "s_waitcnt vmcnt(0) lgkmcnt(0)\n\t"
      "IDLE2_%=:\n\t"
      "s_endpgm\n\t"
      "P3_%=:\n\t"
      "v_cmp_eq_u64_e64 vcc, v[20:21], s[68:69]\n\t"
      "s_and_b32 s42, vcc_lo, s41\n\t"
      "s_cbranch_scc0 NL3_%=\n\t"
      "PN3_%=:\n\t"
      "s_andn2_b32 s41, s41, s42\n\t"
      "s_bitcmp1_b32 s42, 0\n\t"
      "s_cbranch_scc0 C0_3_%=\n\t"
      "buffer_load_dwordx2 v[64:65], v13, s[36:39], 0 offen sc1\n\t"
      "buffer_load_dwordx4 v[66:69], v24, s[36:39], 0 offen offset:0 sc1\n\t"
      "buffer_load_dwordx4 v[70:73], v24, s[36:39], 0 offen offset:1024 sc1\n\t"
      "buffer_load_dwordx4 v[74:77], v25, s[36:39], 0 offen offset:2048 sc1\n\t"
      "C0_3_%=:\n\t"
      "s_bitcmp1_b32 s42, 1\n\t"
      "s_cbranch_scc0 C1_3_%=\n\t"
      "s_mov_b32 s46, 69632\n\t"
      "buffer_load_dwordx2 v[78:79], v13, s[36:39], s46 offen sc1\n\t"
      "buffer_load_dwordx4 v[80:83], v24, s[36:39], s46 offen offset:0 sc1\n\t"
      "buffer_load_dwordx4 v[84:87], v24, s[36:39], s46 offen offset:1024 sc1\n\t"
      "buffer_load_dwordx4 v[88:91], v25, s[36:39], s46 offen offset:2048 sc1\n\t"
      "C1_3_%=:\n\t"
      "s_bitcmp1_b32 s42, 2\n\t"
      "s_cbranch_scc0 C2_3_%=\n\t"
      "s_mov_b32 s46, 139264\n\t"
      "buffer_load_dwordx2 v[92:93], v13, s[36:39], s46 offen sc1\n\t"
      "buffer_load_dwordx4 v[94:97], v24, s[36:39], s46 offen offset:0 sc1\n\t"
      "buffer_load_dwordx4 v[98:101], v24, s[36:39], s46 offen offset:1024 sc1\n\t"
      "buffer_load_dwordx4 v[102:105], v25, s[36:39], s46 offen offset:2048 sc1\n\t"
      "C2_3_%=:\n\t"
      "s_bitcmp1_b32 s42, 3\n\t"
      "s_cbranch_scc0 C3_3_%=\n\t"
      "s_mov_b32 s46, 208896\n\t"
      "buffer_load_dwordx2 v[106:107], v13, s[36:39], s46 offen sc1\n\t"
      "buffer_load_dwordx4 v[108:111], v24, s[36:39], s46 offen offset:0 sc1\n\t"
      "buffer_load_dwordx4 v[112:115], v24, s[36:39], s46 offen offset:1024 sc1\n\t"
      "buffer_load_dwordx4 v[116:119], v25, s[36:39], s46 offen offset:2048 sc1\n\t"
      "C3_3_%=:\n\t"
      "s_bitcmp1_b32 s42, 4\n\t"
      "s_cbranch_scc0 C4_3_%=\n\t"
      "s_mov_b32 s46, 278528\n\t"
      "buffer_load_dwordx2 v[120:121], v13, s[36:39], s46 offen sc1\n\t"
      "buffer_load_dwordx4 v[122:125], v24, s[36:39], s46 offen offset:0 sc1\n\t"
      "buffer_load_dwordx4 v[126:129], v24, s[36:39], s46 offen offset:1024 sc1\n\t"
      "buffer_load_dwordx4 v[130:133], v25, s[36:39], s46 offen offset:2048 sc1\n\t"
      "C4_3_%=:\n\t"
      "s_bitcmp1_b32 s42, 5\n\t"
      "s_cbranch_scc0 C5_3_%=\n\t"
      "s_mov_b32 s46, 348160\n\t"
      "buffer_load_dwordx2 v[134:135], v13, s[36:39], s46 offen sc1\n\t"
      "buffer_load_dwordx4 v[136:139], v24, s[36:39], s46 offen offset:0 sc1\n\t"
      "buffer_load_dwordx4 v[140:143], v24, s[36:39], s46 offen offset:1024 sc1\n\t"
      "buffer_load_dwordx4 v[144:147], v25, s[36:39], s46 offen offset:2048 sc1\n\t"
      "C5_3_%=:\n\t"
      "s_bitcmp1_b32 s42, 6\n\t"
      "s_cbranch_scc0 C6_3_%=\n\t"
      "s_mov_b32 s46, 417792\n\t"
      "buffer_load_dwordx2 v[148:149], v13, s[36:39], s46 offen sc1\n\t"
      "buffer_load_dwordx4 v[150:153], v24, s[36:39], s46 offen offset:0 sc1\n\t"
      "buffer_load_dwordx4 v[154:157], v24, s[36:39], s46 offen offset:1024 sc1\n\t"
      "buffer_load_dwordx4 v[158:161], v25, s[36:39], s46 offen offset:2048 sc1\n\t"
      "C6_3_%=:\n\t"
      "NL3_%=:\n\t"
      "s_cmp_eq_u32 s41, 0\n\t"
      "s_cbranch_scc1 NP3_%=\n\t"
      "global_load_dwordx2 v[20:21], v28, s[84:85] offset:512 sc1\n\t"
      "NP3_%=:\n\t"
      "s_bitcmp1_b32 s43, s44\n\t"
      "s_cbranch_scc0 NB3_%=\n\t"
      "B3_%=:\n\t"
      "s_mul_i32 s45, s44, 14\n\t"
      "s_bitset0_b32 s43, s44\n\t"
      "s_set_gpr_idx_on s45, gpr_idx(SRC1)\n\t"
      "s_bitset0_b32 s58, s70\n\t"
      "v_sub_f32 v12, v18, v64\n\t"
      "s_flbit_i32_b32 s70, s58\n\t"
      "v_exp_f32 v10, v12\n\t"
      "s_xor_b32 s70, s70, 31\n\t"
      "v_lshlrev_b32 v224, 16, v66\n\t"
      "s_and_b32 s44, s70, 15\n\t"
      "v_and_b32 v225, s59, v66\n\t"
      "v_lshlrev_b32 v226, 16, v67\n\t"
      "v_and_b32 v227, s59, v67\n\t"
      "v_lshlrev_b32 v228, 16, v68\n\t"
      "v_and_b32 v229, s59, v68\n\t"
      "v_lshlrev_b32 v230, 16, v69\n\t"
      "v_and_b32 v231, s59, v69\n\t"
      "v_fmac_f32 v19, v10, v65\n\t"
      "v_mov_b32 v11, v10\n\t"
      "v_lshlrev_b32 v232, 16, v70\n\t"
      "v_and_b32 v233, s59, v70\n\t"
      "v_lshlrev_b32 v234, 16, v71\n\t"
      "v_and_b32 v235, s59, v71\n\t"
      "v_lshlrev_b32 v236, 16, v72\n\t"
      "v_and_b32 v237, s59, v72\n\t"
      "v_lshlrev_b32 v238, 16, v73\n\t"
      "v_and_b32 v239, s59, v73\n\t"
      "v_lshlrev_b32 v240, 16, v74\n\t"
      "v_and_b32 v241, s59, v74\n\t"
      "v_lshlrev_b32 v242, 16, v75\n\t"
      "v_and_b32 v243, s59, v75\n\t"
      "v_lshlrev_b32 v244, 16, v76\n\t"
      "v_and_b32 v245, s59, v76\n\t"
      "v_lshlrev_b32 v246, 16, v77\n\t"
      "v_and_b32 v247, s59, v77\n\t"
      "v_permlane32_swap_b32 v10, v11\n\t"
      "s_set_gpr_idx_off\n\t"
      "s_bitcmp1_b32 s43, s44\n\t"
      "v_cndmask_b32_e64 v2, v10, v11, s[60:61]\n\t"
      "v_cndmask_b32_e64 v4, v10, v11, s[62:63]\n\t"
      "v_cndmask_b32_e64 v6, v10, v11, s[64:65]\n\t"
      "v_pk_fma_f32 v[32:33], v[224:225], v[2:3], v[32:33] op_sel_hi:[1,0,1]\n\t"
      "v_pk_fma_f32 v[34:35], v[226:227], v[2:3], v[34:35] op_sel_hi:[1,0,1]\n\t"
      "v_pk_fma_f32 v[36:37], v[228:229], v[2:3], v[36:37] op_sel_hi:[1,0,1]\n\t"
      "v_pk_fma_f32 v[38:39], v[230:231], v[2:3], v[38:39] op_sel_hi:[1,0,1]\n\t"
      "v_pk_fma_f32 v[40:41], v[232:233], v[4:5], v[40:41] op_sel_hi:[1,0,1]\n\t"
      "v_pk_fma_f32 v[42:43], v[234:235], v[4:5], v[42:43] op_sel_hi:[1,0,1]\n\t"
      "v_pk_fma_f32 v[44:45], v[236:237], v[4:5], v[44:45] op_sel_hi:[1,0,1]\n\t"
      "v_pk_fma_f32 v[46:47], v[238:239], v[4:5], v[46:47] op_sel_hi:[1,0,1]\n\t"
      "v_pk_fma_f32 v[48:49], v[240:241], v[6:7], v[48:49] op_sel_hi:[1,0,1]\n\t"
      "v_pk_fma_f32 v[50:51], v[242:243], v[6:7], v[50:51] op_sel_hi:[1,0,1]\n\t"
      "v_pk_fma_f32 v[52:53], v[244:245], v[6:7], v[52:53] op_sel_hi:[1,0,1]\n\t"
      "v_pk_fma_f32 v[54:55], v[246:247], v[6:7], v[54:55] op_sel_hi:[1,0,1]\n\t"
      "s_cbranch_scc1 B3_%=\n\t"
      "NB3_%=:\n\t"
      "s_cmp_eq_u32 s58, 0\n\t"
      "s_cbranch_scc1 FA_%=\n\t"
      "s_or_b32 s43, s43, s42\n\t"
      "s_waitcnt vmcnt(0)\n\t"
      "s_sub_u32 s54, s54, 1\n\t"
      "s_cbranch_scc0 P3_%=\n\t"
      "s_branch TMO_%=\n\t"
      "P4_%=:\n\t"
      "v_cmp_eq_u64_e64 vcc, v[20:21], s[68:69]\n\t"
      "s_and_b32 s42, vcc_lo, s41\n\t"
      "s_cbranch_scc0 NL4_%=\n\t"
      "PN4_%=:\n\t"
      "s_andn2_b32 s41, s41, s42\n\t"
      "s_bitcmp1_b32 s42, 0\n\t"
      "s_cbranch_scc0 C0_4_%=\n\t"
      "buffer_load_dwordx2 v[64:65], v13, s[36:39], 0 offen sc1\n\t"
      "buffer_load_dwordx4 v[66:69], v24, s[36:39], 0 offen offset:0 sc1\n\t"
      "buffer_load_dwordx4 v[70:73], v24, s[36:39], 0 offen offset:1024 sc1\n\t"
      "buffer_load_dwordx4 v[74:77], v24, s[36:39], 0 offen offset:2048 sc1\n\t"
      "buffer_load_dwordx4 v[78:81], v25, s[36:39], 0 offen offset:3072 sc1\n\t"
      "C0_4_%=:\n\t"
      "s_bitcmp1_b32 s42, 1\n\t"
      "s_cbranch_scc0 C1_4_%=\n\t"
      "s_mov_b32 s46, 69632\n\t"
      "buffer_load_dwordx2 v[82:83], v13, s[36:39], s46 offen sc1\n\t"
      "buffer_load_dwordx4 v[84:87], v24, s[36:39], s46 offen offset:0 sc1\n\t"
      "buffer_load_dwordx4 v[88:91], v24, s[36:39], s46 offen offset:1024 sc1\n\t"
      "buffer_load_dwordx4 v[92:95], v24, s[36:39], s46 offen offset:2048 sc1\n\t"
      "buffer_load_dwordx4 v[96:99], v25, s[36:39], s46 offen offset:3072 sc1\n\t"
      "C1_4_%=:\n\t"
      "s_bitcmp1_b32 s42, 2\n\t"
      "s_cbranch_scc0 C2_4_%=\n\t"
      "s_mov_b32 s46, 139264\n\t"
      "buffer_load_dwordx2 v[100:101], v13, s[36:39], s46 offen sc1\n\t"
      "buffer_load_dwordx4 v[102:105], v24, s[36:39], s46 offen offset:0 sc1\n\t"
      "buffer_load_dwordx4 v[106:109], v24, s[36:39], s46 offen offset:1024 sc1\n\t"
      "buffer_load_dwordx4 v[110:113], v24, s[36:39], s46 offen offset:2048 sc1\n\t"
      "buffer_load_dwordx4 v[114:117], v25, s[36:39], s46 offen offset:3072 sc1\n\t"
      "C2_4_%=:\n\t"
      "s_bitcmp1_b32 s42, 3\n\t"
      "s_cbranch_scc0 C3_4_%=\n\t"
      "s_mov_b32 s46, 208896\n\t"
      "buffer_load_dwordx2 v[118:119], v13, s[36:39], s46 offen sc1\n\t"
      "buffer_load_dwordx4 v[120:123], v24, s[36:39], s46 offen offset:0 sc1\n\t"
      "buffer_load_dwordx4 v[124:127], v24, s[36:39], s46 offen offset:1024 sc1\n\t"
      "buffer_load_dwordx4 v[128:131], v24, s[36:39], s46 offen offset:2048 sc1\n\t"
      "buffer_load_dwordx4 v[132:135], v25, s[36:39], s46 offen offset:3072 sc1\n\t"
      "C3_4_%=:\n\t"
      "s_bitcmp1_b32 s42, 4\n\t"
      "s_cbranch_scc0 C4_4_%=\n\t"
      "s_mov_b32 s46, 278528\n\t"
      "buffer_load_dwordx2 v[136:137], v13, s[36:39], s46 offen sc1\n\t"
      "buffer_load_dwordx4 v[138:141], v24, s[36:39], s46 offen offset:0 sc1\n\t"
      "buffer_load_dwordx4 v[142:145], v24, s[36:39], s46 offen offset:1024 sc1\n\t"
      "buffer_load_dwordx4 v[146:149], v24, s[36:39], s46 offen offset:2048 sc1\n\t"
      "buffer_load_dwordx4 v[150:153], v25, s[36:39], s46 offen offset:3072 sc1\n\t"
      "C4_4_%=:\n\t"
      "NL4_%=:\n\t"
      "s_cmp_eq_u32 s41, 0\n\t"
      "s_cbranch_scc1 NP4_%=\n\t"
      "global_load_dwordx2 v[20:21], v28, s[84:85] offset:512 sc1\n\t"
      "NP4_%=:\n\t"
      "s_bitcmp1_b32 s43, s44\n\t"
      "s_cbranch_scc0 NB4_%=\n\t"
      "B4_%=:\n\t"
      "s_mul_i32 s45, s44, 18\n\t"
      "s_bitset0_b32 s43, s44\n\t"
      "s_set_gpr_idx_on s45, gpr_idx(SRC1)\n\t"
      "s_bitset0_b32 s58, s70\n\t"
      "v_sub_f32 v12, v18, v64\n\t"
      "s_flbit_i32_b32 s70, s58\n\t"
      "v_exp_f32 v10, v12\n\t"
      "s_xor_b32 s70, s70, 31\n\t"
      "v_lshlrev_b32 v224, 16, v66\n\t"
      "s_and_b32 s44, s70, 15\n\t"
      "v_and_b32 v225, s59, v66\n\t"
      "v_lshlrev_b32 v226, 16, v67\n\t"
      "v_and_b32 v227, s59, v67\n\t"
      "v_lshlrev_b32 v228, 16, v68\n\t"
      "v_and_b32 v229, s59, v68\n\t"
      "v_lshlrev_b32 v230, 16, v69\n\t"
      "v_and_b32 v231, s59, v69\n\t"
      "v_fmac_f32 v19, v10, v65\n\t"
      "v_mov_b32 v11, v10\n\t"
      "v_lshlrev_b32 v232, 16, v70\n\t"
      "v_and_b32 v233, s59, v70\n\t"
      "v_lshlrev_b32 v234, 16, v71\n\t"
      "v_and_b32 v235, s59, v71\n\t"
      "v_lshlrev_b32 v236, 16, v72\n\t"
      "v_and_b32 v237, s59, v72\n\t"
      "v_lshlrev_b32 v238, 16, v73\n\t"
      "v_and_b32 v239, s59, v73\n\t"
      "v_lshlrev_b32 v240, 16, v74\n\t"
      "v_and_b32 v241, s59, v74\n\t"
      "v_lshlrev_b32 v242, 16, v75\n\t"
      "v_and_b32 v243, s59, v75\n\t"
      "v_lshlrev_b32 v244, 16, v76\n\t"
      "v_and_b32 v245, s59, v76\n\t"
      "v_lshlrev_b32 v246, 16, v77\n\t"
      "v_and_b32 v247, s59, v77\n\t"
      "v_lshlrev_b32 v248, 16, v78\n\t"
      "v_and_b32 v249, s59, v78\n\t"
      "v_lshlrev_b32 v250, 16, v79\n\t"
      "v_and_b32 v251, s59, v79\n\t"
      "v_lshlrev_b32 v252, 16, v80\n\t"
      "v_and_b32 v253, s59, v80\n\t"
      "v_lshlrev_b32 v254, 16, v81\n\t"
      "v_and_b32 v255, s59, v81\n\t"
      "v_permlane32_swap_b32 v10, v11\n\t"
      "s_set_gpr_idx_off\n\t"
      "s_bitcmp1_b32 s43, s44\n\t"
      "v_cndmask_b32_e64 v2, v10, v11, s[60:61]\n\t"
      "v_cndmask_b32_e64 v4, v10, v11, s[62:63]\n\t"
      "v_cndmask_b32_e64 v6, v10, v11, s[64:65]\n\t"
      "v_cndmask_b32_e64 v8, v10, v11, s[66:67]\n\t"
      "v_pk_fma_f32 v[32:33], v[224:225], v[2:3], v[32:33] op_sel_hi:[1,0,1]\n\t"
      "v_pk_fma_f32 v[34:35], v[226:227], v[2:3], v[34:35] op_sel_hi:[1,0,1]\n\t"
      "v_pk_fma_f32 v[36:37], v[228:229], v[2:3], v[36:37] op_sel_hi:[1,0,1]\n\t"
      "v_pk_fma_f32 v[38:39], v[230:231], v[2:3], v[38:39] op_sel_hi:[1,0,1]\n\t"
      "v_pk_fma_f32 v[40:41], v[232:233], v[4:5], v[40:41] op_sel_hi:[1,0,1]\n\t"
      "v_pk_fma_f32 v[42:43], v[234:235], v[4:5], v[42:43] op_sel_hi:[1,0,1]\n\t"
      "v_pk_fma_f32 v[44:45], v[236:237], v[4:5], v[44:45] op_sel_hi:[1,0,1]\n\t"
      "v_pk_fma_f32 v[46:47], v[238:239], v[4:5], v[46:47] op_sel_hi:[1,0,1]\n\t"
      "v_pk_fma_f32 v[48:49], v[240:241], v[6:7], v[48:49] op_sel_hi:[1,0,1]\n\t"
      "v_pk_fma_f32 v[50:51], v[242:243], v[6:7], v[50:51] op_sel_hi:[1,0,1]\n\t"
      "v_pk_fma_f32 v[52:53], v[244:245], v[6:7], v[52:53] op_sel_hi:[1,0,1]\n\t"
      "v_pk_fma_f32 v[54:55], v[246:247], v[6:7], v[54:55] op_sel_hi:[1,0,1]\n\t"
      "v_pk_fma_f32 v[56:57], v[248:249], v[8:9], v[56:57] op_sel_hi:[1,0,1]\n\t"
      "v_pk_fma_f32 v[58:59], v[250:251], v[8:9], v[58:59] op_sel_hi:[1,0,1]\n\t"
      "v_pk_fma_f32 v[60:61], v[252:253], v[8:9], v[60:61] op_sel_hi:[1,0,1]\n\t"
      "v_pk_fma_f32 v[62:63], v[254:255], v[8:9], v[62:63] op_sel_hi:[1,0,1]\n\t"
      "s_cbranch_scc1 B4_%=\n\t"
      "NB4_%=:\n\t"
      "s_cmp_eq_u32 s58, 0\n\t"
      "s_cbranch_scc1 FA_%=\n\t"
      "s_or_b32 s43, s43, s42\n\t"
      "s_waitcnt vmcnt(0)\n\t"
      "s_sub_u32 s54, s54, 1\n\t"
      "s_cbranch_scc0 P4_%=\n\t"
      "s_branch TMO_%=\n\t"
      "TMO_%=:\n\t"
      "s_waitcnt vmcnt(0)\n\t"
      "v_mov_b32 v19, 0x7fc00000\n\t"
      "s_branch FB_%=\n\t"
      "FIN_%=:\n\t"
      "s_waitcnt vmcnt(0)\n\t"
      "s_barrier\n\t"
      "GEN_%=:\n\t"
      "s_mov_b32 %[omode], -1\n\t"
      "s_branch END_%=\n\t"
      "MKF0_%=:\n\t"
      "v_cmp_ge_i32_e64 s[60:61], 0, v21\n\t"
      "v_cmp_ge_i32_e64 s[62:63], 1, v21\n\t"
      "v_cmp_ge_i32_e64 s[64:65], 2, v21\n\t"
      "v_cmp_ge_i32_e64 s[66:67], 3, v21\n\t"
      "s_nop 2\n\t"
      "v_cndmask_b32_e64 v64, v64, v20, s[60:61]\n\t"
      "v_cndmask_b32_e64 v65, v65, v20, s[62:63]\n\t"
      "v_cndmask_b32_e64 v66, v66, v20, s[64:65]\n\t"
      "v_cndmask_b32_e64 v67, v67, v20, s[66:67]\n\t"
      "v_cndmask_b32_e64 v72, v72, v20, s[60:61]\n\t"
      "v_cndmask_b32_e64 v73, v73, v20, s[62:63]\n\t"
      "v_cndmask_b32_e64 v74, v74, v20, s[64:65]\n\t"
      "v_cndmask_b32_e64 v75, v75, v20, s[66:67]\n\t"
      "v_cndmask_b32_e64 v80, v80, v20, s[60:61]\n\t"
      "v_cndmask_b32_e64 v81, v81, v20, s[62:63]\n\t"
      "v_cndmask_b32_e64 v82, v82, v20, s[64:65]\n\t"
      "v_cndmask_b32_e64 v83, v83, v20, s[66:67]\n\t"
      "v_cndmask_b32_e64 v88, v88, v20, s[60:61]\n\t"
      "v_cndmask_b32_e64 v89, v89, v20, s[62:63]\n\t"
      "v_cndmask_b32_e64 v90, v90, v20, s[64:65]\n\t"
      "v_cndmask_b32_e64 v91, v91, v20, s[66:67]\n\t"
      "v_cmp_ge_i32_e64 s[60:61], 16, v21\n\t"
      "v_cmp_ge_i32_e64 s[62:63], 17, v21\n\t"
      "v_cmp_ge_i32_e64 s[64:65], 18, v21\n\t"
      "v_cmp_ge_i32_e64 s[66:67], 19, v21\n\t"
      "v_cndmask_b32_e64 v68, v68, v20, s[60:61]\n\t"
      "v_cndmask_b32_e64 v69, v69, v20, s[62:63]\n\t"
      "v_cndmask_b32_e64 v70, v70, v20, s[64:65]\n\t"
      "v_cndmask_b32_e64 v71, v71, v20, s[66:67]\n\t"
      "v_cndmask_b32_e64 v76, v76, v20, s[60:61]\n\t"
      "v_cndmask_b32_e64 v77, v77, v20, s[62:63]\n\t"
      "v_cndmask_b32_e64 v78, v78, v20, s[64:65]\n\t"
      "v_cndmask_b32_e64 v79, v79, v20, s[66:67]\n\t"
      "v_cndmask_b32_e64 v84, v84, v20, s[60:61]\n\t"
      "v_cndmask_b32_e64 v85, v85, v20, s[62:63]\n\t"
      "v_cndmask_b32_e64 v86, v86, v20, s[64:65]\n\t"
      "v_cndmask_b32_e64 v87, v87, v20, s[66:67]\n\t"
      "v_cndmask_b32_e64 v92, v92, v20, s[60:61]\n\t"
      "v_cndmask_b32_e64 v93, v93, v20, s[62:63]\n\t"
      "v_cndmask_b32_e64 v94, v94, v20, s[64:65]\n\t"
      "v_cndmask_b32_e64 v95, v95, v20, s[66:67]\n\t"
      "s_branch MBF0_%=\n\t"
      "MKD1_%=:\n\t"
      "v_cmp_ge_i32_e64 s[60:61], 32, v21\n\t"
      "v_cmp_ge_i32_e64 s[62:63], 33, v21\n\t"
      "v_cmp_ge_i32_e64 s[64:65], 34, v21\n\t"
      "v_cmp_ge_i32_e64 s[66:67], 35, v21\n\t"
      "s_nop 2\n\t"
      "v_cndmask_b32_e64 v96, v96, v20, s[60:61]\n\t"
      "v_cndmask_b32_e64 v97, v97, v20, s[62:63]\n\t"
      "v_cndmask_b32_e64 v98, v98, v20, s[64:65]\n\t"
      "v_cndmask_b32_e64 v99, v99, v20, s[66:67]\n\t"
      "v_cndmask_b32_e64 v104, v104, v20, s[60:61]\n\t"
      "v_cndmask_b32_e64 v105, v105, v20, s[62:63]\n\t"
      "v_cndmask_b32_e64 v106, v106, v20, s[64:65]\n\t"
      "v_cndmask_b32_e64 v107, v107, v20, s[66:67]\n\t"
      "v_cndmask_b32_e64 v112, v112, v20, s[60:61]\n\t"
      "v_cndmask_b32_e64 v113, v113, v20, s[62:63]\n\t"
      "v_cndmask_b32_e64 v114, v114, v20, s[64:65]\n\t"
      "v_cndmask_b32_e64 v115, v115, v20, s[66:67]\n\t"
      "v_cndmask_b32_e64 v120, v120, v20, s[60:61]\n\t"
      "v_cndmask_b32_e64 v121, v121, v20, s[62:63]\n\t"
      "v_cndmask_b32_e64 v122, v122, v20, s[64:65]\n\t"
      "v_cndmask_b32_e64 v123, v123, v20, s[66:67]\n\t"
      "v_cmp_ge_i32_e64 s[60:61], 48, v21\n\t"
      "v_cmp_ge_i32_e64 s[62:63], 49, v21\n\t"
      "v_cmp_ge_i32_e64 s[64:65], 50, v21\n\t"
      "v_cmp_ge_i32_e64 s[66:67], 51, v21\n\t"
      "v_cndmask_b32_e64 v100, v100, v20, s[60:61]\n\t"
      "v_cndmask_b32_e64 v101, v101, v20, s[62:63]\n\t"
      "v_cndmask_b32_e64 v102, v102, v20, s[64:65]\n\t"
      "v_cndmask_b32_e64 v103, v103, v20, s[66:67]\n\t"
      "v_cndmask_b32_e64 v108, v108, v20, s[60:61]\n\t"
      "v_cndmask_b32_e64 v109, v109, v20, s[62:63]\n\t"
      "v_cndmask_b32_e64 v110, v110, v20, s[64:65]\n\t"
      "v_cndmask_b32_e64 v111, v111, v20, s[66:67]\n\t"
      "v_cndmask_b32_e64 v116, v116, v20, s[60:61]\n\t"
      "v_cndmask_b32_e64 v117, v117, v20, s[62:63]\n\t"
      "v_cndmask_b32_e64 v118, v118, v20, s[64:65]\n\t"
      "v_cndmask_b32_e64 v119, v119, v20, s[66:67]\n\t"
      "v_cndmask_b32_e64 v124, v124, v20, s[60:61]\n\t"
      "v_cndmask_b32_e64 v125, v125, v20, s[62:63]\n\t"
      "v_cndmask_b32_e64 v126, v126, v20, s[64:65]\n\t"
      "v_cndmask_b32_e64 v127, v127, v20, s[66:67]\n\t"
      "s_branch MBD1_%=\n\t"
      "END_%=:\n\t"
      : [ob] "=&s"(b), [on] "=&s"(n), [oj] "=&s"(j), [olen] "=&s"(len), [oslot] "=&s"(slot), [ont] "=&s"(nt), [oe0] "=&s"(e0), [oe1] "=&s"(e1),
        [ob0] "=&s"(b0), [ob1] "=&s"(b1), [ob2] "=&s"(b2), [ob3] "=&s"(b3), [omode] "=&s"(mode)
      : [ka] "s"(__builtin_amdgcn_kernarg_segment_ptr()), [bid] "s"(blockIdx.x), [tid] "v"(threadIdx.x)
      : "v1", "v2", "v3", "v4", "v5", "v6", "v7", "v8", "v9", "v10", "v11", "v12", "v13", "v14", "v15", "v16", "v17", "v18", "v19", "v20", "v21", "v22", "v23",
        "v24", "v25", "v26", "v27", "v28", "v29", "v30", "v31", "v32", "v33", "v34", "v35", "v36", "v37", "v38", "v39", "v40", "v41", "v42", "v43", "v44",
        "v45", "v46", "v47", "v48", "v49", "v50", "v51", "v52", "v53", "v54", "v55", "v56", "v57", "v58", "v59", "v60", "v61", "v62", "v63", "v64", "v65",
        "v66", "v67", "v68", "v69", "v70", "v71", "v72", "v73", "v74", "v75", "v76", "v77", "v78", "v79", "v80", "v81", "v82", "v83", "v84", "v85", "v86",
        "v87", "v88", "v89", "v90", "v91", "v92", "v93", "v94", "v95", "v96", "v97", "v98", "v99", "v100", "v101", "v102", "v103", "v104", "v105", "v106",
        "v107", "v108", "v109", "v110", "v111", "v112", "v113", "v114", "v115", "v116", "v117", "v118", "v119", "v120", "v121", "v122", "v123", "v124", "v125",
        "v126", "v127", "v128", "v129", "v130", "v131", "v132", "v133", "v134", "v135", "v136", "v137", "v138", "v139", "v140", "v141", "v142", "v143", "v144",
        "v145", "v146", "v147", "v148", "v149", "v150", "v151", "v152", "v153", "v154", "v155", "v156", "v157", "v158", "v159", "v160", "v161", "v162", "v163",
        "v164", "v165", "v166", "v167", "v168", "v169", "v170", "v171", "v172", "v173", "v174", "v175", "v176", "v177", "v178", "v179", "v180", "v181", "v182",
        "v183", "v184", "v185", "v186", "v187", "v188", "v189", "v190", "v191", "v192", "v193", "v194", "v195", "v196", "v197", "v198", "v199", "v200", "v201",
        "v202", "v203", "v204", "v205", "v206", "v207", "v208", "v209", "v210", "v211", "v212", "v213", "v214", "v215", "v216", "v217", "v218", "v219", "v220",
        "v221", "v222", "v223", "v224", "v225", "v226", "v227", "v228", "v229", "v230", "v231", "v232", "v233", "v234", "v235", "v236", "v237", "v238", "v239",
        "v240", "v241", "v242", "v243", "v244", "v245", "v246", "v247", "v248", "v249", "v250", "v251", "v252", "v253", "v254", "v255", "a0", "a1", "a2", "a3",
        "a4", "a5", "a6", "a7", "a8", "a9", "a10", "a11", "a12", "a13", "a14", "a15", "a16", "a17", "a18", "a19", "a20", "a21", "a22", "a23", "a24", "a25",
        "a26", "a27", "a28", "a29", "a30", "a31", "a32", "a33", "a34", "a35", "a36", "a37", "a38", "a39", "a40", "a41", "a42", "a43", "a44", "a45", "a46",
        "a47", "a48", "a49", "a50", "a51", "a52", "a53", "a54", "a55", "a56", "a57", "a58", "a59", "a60", "a61", "a62", "a63", "a64", "a65", "a66", "a67",
        "a68", "a69", "a70", "a71", "a72", "a73", "a74", "a75", "a76", "a77", "a78", "a79", "a80", "a81", "a82", "a83", "a84", "a85", "a86", "a87", "a88",
        "a89", "a90", "a91", "a92", "a93", "a94", "a95", "a96", "a97", "a98", "a99", "a100", "a101", "a102", "a103", "a104", "a105", "a106", "a107", "a108",
        "a109", "a110", "a111", "a112", "a113", "a114", "a115", "a116", "a117", "a118", "a119", "a120", "a121", "a122", "a123", "a124", "a125", "a126", "a127",
        "a128", "a129", "a130", "a131", "a132", "a133", "a134", "a135", "a136", "a137", "a138", "a139", "a140", "a141", "a142", "a143", "a144", "a145", "a146",
        "a147", "a148", "a149", "a150", "a151", "a152", "a153", "a154", "a155", "a156", "a157", "a158", "a159", "a160", "a161", "a162", "a163", "a164", "a165",
        "a166", "a167", "a168", "a169", "a170", "a171", "a172", "a173", "a174", "a175", "a176", "a177", "a178", "a179", "a180", "a181", "a182", "a183", "a184",
        "a185", "a186", "a187", "a188", "a189", "a190", "a191", "a192", "a193", "a194", "a195", "a196", "a197", "a198", "a199", "s36", "s37", "s38", "s39",
        "s40", "s41", "s42", "s43", "s44", "s45", "s46", "s47", "s48", "s49", "s50", "s51", "s52", "s53", "s54", "s55", "s56", "s57", "s58", "s59", "s60",
        "s61", "s62", "s63", "s64", "s65", "s66", "s67", "s68", "s69", "s70", "s71", "s72", "s73", "s74", "s75", "s76", "s77", "s78", "s79", "s80", "s81",
        "s82", "s83", "s84", "s85", "s86", "s87", "s88", "s89", "s90", "s91", "s92", "s93", "s94", "s95", "s96", "s97", "s98", "s99", "vcc", "scc", "memory");
  const unsigned lane = threadIdx.x & 63u, wv = rfl(threadIdx.x >> 6), h = (blockIdx.x >> HSH) & 3u, g = h * 8u + (unsigned)b;
  bool ok = false;
  unsigned i0 = (unsigned)mode, cnt = 4u;
  if (mode < 0) {
    if (nt > 0) {
      const unsigned long long tag = (((unsigned long long)e1 << 32) | e0) + 0x9e3779b97f4a7c15ull;
      gu64* flg = (gu64*)(ws + FLG_OFF + g * 2048u + 512u);
      if (threadIdx.x == 0) __hip_atomic_store(flg + j, tag, RLX_AGENT);
      for (unsigned spins = 0; spins < (1u << 22); ++spins) {
        const unsigned long long v = (int)lane < n ? __hip_atomic_load(flg + lane, RLX_AGENT) : tag;
        if (__all(v == tag)) { ok = true; break; }
        __builtin_amdgcn_s_sleep(2);
      }
      asm volatile("" ::: "memory");
      if (ok && threadIdx.x == 0) __hip_atomic_store((gu64*)(ws + FLG_OFF + g * 2048u), tag, RLX_AGENT);
    }
    const unsigned um = (unsigned)__ballot(lane < 16u && (int)(((4u * lane + wv) * (unsigned)n) >> 6) == j);
    i0 = um ? (unsigned)__builtin_ctz(um) : 0u; cnt = (unsigned)__builtin_popcount(um);
  }
  const __amdgpu_buffer_rsrc_t srd = __builtin_amdgcn_make_buffer_rsrc((void*)(ws + (size_t)((unsigned)slot * SLOT_PART + wv * WAVE_PART)), (short)0, ok ? (int)((unsigned)n * SLOT_PART) : 0, 0x00020000);
  const size_t row0 = (size_t)(b * 32 + (int)h * 8) * 32 + wv * 64u;
#pragma unroll 1
  for (unsigned k = 0; k < cnt; ++k) {
    const unsigned i = i0 + k;
    unsigned m = mode < 0 ? combine_unit(srd, n, i, lane, !ok, out + row0 * D) : (k == 0 ? b0 : (k == 1 ? b1 : (k == 2 ? b2 : b3))) & 0xffffu;
    while (m) {
      const int r = __builtin_ctz(m); m &= m - 1u;
      slow_row(q, kc, vc, bt, len, b, (int)h, row0 + (i >> 2) * 16u + (unsigned)r, out, (int)lane, (int)((i & 3u) * 32u));
    }
  }
}


extern "C" void kernel_launch(void* const* d_in, const int* in_sizes, int n_in, void* d_out, int out_size, void* d_ws, size_t ws_size, hipStream_t stream) {
  (void)hipFuncSetAttribute((const void*)attn_fused, hipFuncAttributeMaxDynamicSharedMemorySize, (int)LDS_BYTES);
  attn_fused<<<256, 256, LDS_BYTES, stream>>>((const unsigned short*)d_in[0], (const unsigned short*)d_in[1], (const unsigned short*)d_in[2], (const int*)d_in[3], (const int*)d_in[4],
                                               (char*)d_ws, (unsigned short*)d_out);
}
```
